```python
import torch

def _softmax(x, axis=-1):
    return torch.softmax(x, dim=axis)
def _gelu(x):
    return torch.nn.functional.gelu(x, approximate="tanh")
def _silu(x):
    return torch.nn.functional.silu(x)
def _sigmoid(x):
    return torch.sigmoid(x)
def _layernorm(x, axis=-1, eps=1e-5):
    m = x.mean(dim=axis, keepdim=True)
    v = x.var(dim=axis, keepdim=True, unbiased=False)
    return (x - m) / torch.sqrt(v + eps)
def _scatter_add(tgt, idx, src, axis=0):
    return tgt.clone().index_add_(axis, idx, src)
def _segment_sum(src, seg, n):
    out = torch.zeros((n,) + src.shape[1:], dtype=src.dtype)
    return out.index_add_(0, seg, src)
def _pad(x, pw):
    flat = tuple(v for pair in reversed(pw) for v in pair)
    return torch.nn.functional.pad(x, flat)

kernel_name = 'gated_mix__grouped_kv_mqa__glu_gated_geglu__prompt'

def setup_inputs():

    _w = (lambda a: (lambda t: t.to(torch.bfloat16) if t.is_floating_point() else t)(torch.as_tensor(a)))
    def _A_setup():
        B, S, D, H = 1, 2048, 640, 32
        Dh = D // H
        return {
            "x": _w((lambda *s: torch.randn(*s, dtype=torch.bfloat16) * 0.05)(B, S, D)),
            "wq": _w((lambda *s: torch.randn(*s, dtype=torch.bfloat16) * 0.8)(D, H * Dh)),
            "wk": _w((lambda *s: torch.randn(*s, dtype=torch.bfloat16) * 0.8)(D, Dh)),
            "wv": _w((lambda *s: torch.randn(*s, dtype=torch.bfloat16) * 0.05)(D, Dh)),
            "wo": _w((lambda *s: torch.randn(*s, dtype=torch.bfloat16) * 0.05)(H * Dh, D)),
        }
    _A_in = _A_setup()

    def _B_setup():
        B, S, Dm, Df = 1, 2048, 640, 1536
        return {
            "x": _w((lambda *s: torch.randn(*s, dtype=torch.bfloat16) * 0.05)(B, S, Dm)),
            "w_gate": _w((lambda *s: torch.randn(*s, dtype=torch.bfloat16) * 0.8)(Dm, Df) * (Dm ** -0.5)),
            "w_up": _w((lambda *s: torch.randn(*s, dtype=torch.bfloat16) * 0.8)(Dm, Df) * (Dm ** -0.5)),
            "w_down": _w((lambda *s: torch.randn(*s, dtype=torch.bfloat16) * 0.8)(Df, Dm) * (Df ** -0.5)),
        }
    _B_in = _B_setup()

    return {
        "A_x": _A_in["x"],
        "A_wq": _A_in["wq"],
        "A_wk": _A_in["wk"],
        "A_wv": _A_in["wv"],
        "A_wo": _A_in["wo"],
        "B_x": _B_in["x"],
        "B_w_gate": _B_in["w_gate"],
        "B_w_up": _B_in["w_up"],
        "B_w_down": _B_in["w_down"],
        "gate_w": _w((lambda *s: torch.randn(*s, dtype=torch.bfloat16) * 0.05)(640) * 16.0),
    }

def reference(A_x, A_wq, A_wk, A_wv, A_wo, B_x, B_w_gate, B_w_up, B_w_down, gate_w):
    def _A_ref(x, wq, wk, wv, wo):
        B, S, D = x.shape
        H = 32
        Dh = D // H
        q = (lambda x, *d: x.permute(*d))((x @ wq).reshape(B, S, H, Dh), 0, 2, 1, 3)
        k = x @ wk
        v = x @ wv
        scores = torch.einsum("bhqd,bkd->bhqk", q, k) * (Dh ** -0.5)
        p = _softmax(scores, axis=-1)
        o = torch.einsum("bhqk,bkd->bhqd", p, v)
        return (lambda x, *d: x.permute(*d))(o, 0, 2, 1, 3).reshape(B, S, H * Dh) @ wo

    def _B_ref(x, w_gate, w_up, w_down):
        g = torch.einsum("bsd,df->bsf", x, w_gate)
        u = torch.einsum("bsd,df->bsf", x, w_up)
        h = _gelu(g) * u
        return torch.einsum("bsf,fd->bsd", h, w_down)

    x = A_x
    g = _sigmoid((x * gate_w).sum(-1, keepdims=True))
    a = _A_ref(x, A_wq, A_wk, A_wv, A_wo)
    b = _B_ref(x, B_w_gate, B_w_up, B_w_down)
    return g * a + (1 - g) * b
```

```cpp
#include <hip/hip_runtime.h>
#include <hip/hip_bf16.h>
typedef unsigned short u16;
__device__ __forceinline__ float b2f(u16 b) { return __uint_as_float(((unsigned)b) << 16); }
__device__ __forceinline__ u16 f2b(float f) { unsigned u = __float_as_uint(f); u += 0x7fffu + ((u >> 16) & 1u); return (u16)(u >> 16); }
constexpr int S = 2048, D = 640, H = 32, DH = 20, DF = 1536;
__global__ void k_qkv(const u16* x, const u16* wq, const u16* wk, const u16* wv, const u16* gw, u16* q, u16* k, u16* v, float* g) {
    int idx = blockIdx.x * blockDim.x + threadIdx.x; int t = idx / 681, c = idx % 681; if (t >= S) return;
    const u16* xr = x + t * D; float acc = 0;
    if (c < 640) { for (int i = 0; i < D; ++i) acc += b2f(xr[i]) * b2f(wq[i * 640 + c]); q[t * 640 + c] = f2b(acc); }
    else if (c < 660) { for (int i = 0; i < D; ++i) acc += b2f(xr[i]) * b2f(wk[i * 20 + c - 640]); k[t * 20 + c - 640] = f2b(acc); }
    else if (c < 680) { for (int i = 0; i < D; ++i) acc += b2f(xr[i]) * b2f(wv[i * 20 + c - 660]); v[t * 20 + c - 660] = f2b(acc); }
    else { for (int i = 0; i < D; ++i) acc += b2f(f2b(b2f(xr[i]) * b2f(gw[i]))); float s = b2f(f2b(acc)); g[t] = b2f(f2b(1.f / (1.f + __expf(-s)))); }
}
__global__ void k_attn(const u16* q, const u16* k, const u16* v, u16* o) {
    int idx = blockIdx.x * blockDim.x + threadIdx.x; int t = idx / H, h = idx % H;
    float qr[DH], acc[DH]; for (int d = 0; d < DH; ++d) { qr[d] = b2f(q[t * 640 + h * DH + d]); acc[d] = 0; }
    float m = -1e30f, l = 0;
    for (int j = 0; j < S; ++j) { float s = 0; for (int d = 0; d < DH; ++d) s += qr[d] * b2f(k[j * DH + d]); s *= 0.22360679f;
        float mn = fmaxf(m, s), a = __expf(m - mn), p = __expf(s - mn); l = l * a + p; for (int d = 0; d < DH; ++d) acc[d] = acc[d] * a + p * b2f(v[j * DH + d]); m = mn; }
    for (int d = 0; d < DH; ++d) o[t * 640 + h * DH + d] = f2b(acc[d] / l);
}
__global__ void k_up(const u16* x, const u16* wg, const u16* wu, u16* hh) {
    int idx = blockIdx.x * blockDim.x + threadIdx.x; int t = idx / DF, c = idx % DF;
    const u16* xr = x + t * D; float a = 0, b = 0; for (int i = 0; i < D; ++i) { float xv = b2f(xr[i]); a += xv * b2f(wg[i * DF + c]); b += xv * b2f(wu[i * DF + c]); }
    a = b2f(f2b(a)); b = b2f(f2b(b));
    float ge = 0.5f * a * (1.f + tanhf(0.7978845608f * (a + 0.044715f * a * a * a))); ge = b2f(f2b(ge));
    hh[idx] = f2b(ge * b);
}
__global__ void k_out(const u16* o, const u16* wo, const u16* hh, const u16* wd, const float* g, u16* out) {
    int idx = blockIdx.x * blockDim.x + threadIdx.x; int t = idx / D, c = idx % D;
    float a = 0, b = 0; for (int i = 0; i < D; ++i) a += b2f(o[t * D + i]) * b2f(wo[i * D + c]);
    for (int i = 0; i < DF; ++i) b += b2f(hh[t * DF + i]) * b2f(wd[i * D + c]);
    float gg = g[t]; out[idx] = f2b(gg * a + (1.f - gg) * b);
}
extern "C" void kernel_launch(void* const* d_in, const int* in_sizes, int n_in, void* d_out, int out_size, void* d_ws, size_t ws_size, hipStream_t stream) {
    const u16* x = (const u16*)d_in[0]; const u16* wq = (const u16*)d_in[1]; const u16* wk = (const u16*)d_in[2]; const u16* wv = (const u16*)d_in[3]; const u16* wo = (const u16*)d_in[4];
    const u16* wg = (const u16*)d_in[6]; const u16* wu = (const u16*)d_in[7]; const u16* wd = (const u16*)d_in[8]; const u16* gw = (const u16*)d_in[9];
    char* ws = (char*)d_ws; u16* q = (u16*)ws; u16* k = (u16*)(ws + (4 << 20)); u16* v = (u16*)(ws + (5 << 20)); float* g = (float*)(ws + (6 << 20)); u16* o = (u16*)(ws + (8 << 20)); u16* hh = (u16*)(ws + (16 << 20));
    k_qkv<<<(S * 681 + 255) / 256, 256, 0, stream>>>(x, wq, wk, wv, gw, q, k, v, g);
    k_attn<<<S * H / 256, 256, 0, stream>>>(q, k, v, o);
    k_up<<<S * DF / 256, 256, 0, stream>>>(x, wg, wu, hh);
    k_out<<<S * D / 256, 256, 0, stream>>>(o, wo, hh, wd, g, (u16*)d_out);
}
```

```cpp
#include <hip/hip_runtime.h>
#include <hip/hip_bf16.h>
#include <stdint.h>
#include <type_traits>

typedef unsigned short u16;
typedef unsigned long long u64;
typedef __attribute__((ext_vector_type(8))) __bf16 bf16x8;
typedef __attribute__((ext_vector_type(16))) __bf16 bf16x16;
typedef __attribute__((ext_vector_type(4))) short s16x4;
typedef __attribute__((ext_vector_type(4))) float f32x4;
typedef __attribute__((ext_vector_type(8))) float f32x8;
typedef __attribute__((ext_vector_type(16))) float f32x16;
typedef __attribute__((ext_vector_type(2))) unsigned u32x2;
typedef __attribute__((ext_vector_type(4))) unsigned u32x4;
#define LAS __attribute__((address_space(3)))
#define GAS __attribute__((address_space(1)))
#define DI __device__ __forceinline__

constexpr int D_ = 640;
constexpr int KV_CHUNK = 22528, KV_K2 = 8192, KV_V = 10240, KV_VT = 1536;
constexpr size_t WS_KV = 0, WS_G = 0x30000, WS_FL = 0x40000, WS_O = 4u << 20, WS_H = 8u << 20;
constexpr int FS = 256, FL_CH = 1024, FL_KV = 4096, FL_H = FL_KV + 256 * FS, FL_O = FL_H + 256 * FS;
constexpr float QSCALE = 0.22360679774997896f * 1.4426950408889634f;
constexpr int LDS_BYTES = 81920;
#define G_PRIO 0
#define UP_PRIO 0
#define A_PRIO 1
#define POLL_SLEEP 4
#define G_DELAY 0

#define STAMP(k)
#define SARG
#define SPASS

template <int N> DI void wait_vm() { asm volatile("s_waitcnt vmcnt(%0)" ::"n"(N) : "memory"); }
DI void wait_lgkm0() { asm volatile("s_waitcnt lgkmcnt(0)" ::: "memory"); }
template <int N> DI void wait_bar() { asm volatile("s_waitcnt vmcnt(%0) lgkmcnt(0)\n\ts_barrier" ::"n"(N) : "memory"); }
DI void bar() { asm volatile("s_waitcnt lgkmcnt(0)\n\ts_barrier" ::: "memory"); }
DI void glds(const void* sbase, unsigned voff, unsigned lds) {
    asm volatile("s_mov_b32 m0, %2\n\ts_nop 0\n\tglobal_load_lds_dwordx4 %0, %1" ::"v"(voff), "s"(sbase), "s"(lds) : "memory");
}
DI void glds_sc1(const void* sbase, unsigned voff, unsigned lds) {
    asm volatile("s_mov_b32 m0, %2\n\ts_nop 0\n\tglobal_load_lds_dwordx4 %0, %1 sc1" ::"v"(voff), "s"(sbase), "s"(lds) : "memory");
}
DI void set_m0(unsigned lds) { asm volatile("s_mov_b32 m0, %0\n\ts_nop 0" ::"s"(lds) : "memory"); }
template <int OFF> DI void dma(const void* sbase, unsigned voff) { asm volatile("global_load_lds_dwordx4 %0, %1 offset:%2" ::"v"(voff), "s"(sbase), "n"(OFF) : "memory"); }
#define SBAR() __builtin_amdgcn_sched_barrier(0)
DI void st16(char* sbase, unsigned voff, u32x4 v) { asm volatile("global_store_dwordx4 %0, %1, %2 sc1\n\ts_nop 1" ::"v"(voff), "v"(v), "s"(sbase) : "memory"); }
DI void st8(char* sbase, unsigned voff, u32x2 v) { asm volatile("global_store_dwordx2 %0, %1, %2 sc1" ::"v"(voff), "v"(v), "s"(sbase) : "memory"); }
DI void st4(char* sbase, unsigned voff, unsigned v) { asm volatile("global_store_dword %0, %1, %2 sc1" ::"v"(voff), "v"(v), "s"(sbase) : "memory"); }
DI float ld4_sc1(const char* sbase, unsigned voff) { float r; asm volatile("global_load_dword %0, %1, %2 sc1\n\ts_waitcnt vmcnt(0)" : "=&v"(r) : "v"(voff), "s"(sbase) : "memory"); return r; }
DI unsigned cvtpk(float lo, float hi) { unsigned r; asm("v_cvt_pk_bf16_f32 %0, %1, %2" : "=v"(r) : "v"(lo), "v"(hi)); return r; }
DI s16x4 trd(unsigned a) { return __builtin_amdgcn_ds_read_tr16_b64_v4i16((LAS s16x4*)a); }
DI bf16x8 cat(s16x4 a, s16x4 b) { return __builtin_bit_cast(bf16x8, __builtin_shufflevector(a, b, 0, 1, 2, 3, 4, 5, 6, 7)); }
DI bf16x8 lds128(unsigned a) { return *(LAS bf16x8*)a; }
DI f32x4 mfma16(bf16x8 a, bf16x8 b, f32x4 c) { return __builtin_amdgcn_mfma_f32_16x16x32_bf16(a, b, c, 0, 0, 0); }
DI f32x16 mfma32(bf16x8 a, bf16x8 b, f32x16 c) { return __builtin_amdgcn_mfma_f32_32x32x16_bf16(a, b, c, 0, 0, 0); }
DI f32x16 mfma32k8(u32x2 a, u32x2 b, f32x16 c) { return __builtin_amdgcn_mfma_f32_32x32x8bf16_1k(__builtin_bit_cast(s16x4, a), __builtin_bit_cast(s16x4, b), c, 0, 0, 0); }
DI f32x16 smfmac(bf16x8 a, f32x8 b, f32x16 c, int idx) { return __builtin_amdgcn_smfmac_f32_32x32x32_bf16(a, __builtin_bit_cast(bf16x16, b), c, idx, 0, 0); }
template <int I, int N, class F> DI void sfor(F&& f) { if constexpr (I < N) { f(std::integral_constant<int, I>{}); sfor<I + 1, N>(f); } }
DI int krow_of(int rho) { const int g = ((rho >> 4) << 1) | ((rho >> 2) & 1); return 8 * g + 4 * ((rho >> 3) & 1) + (rho & 3); }
DI int mod10(int v) { return v >= 10 ? v - 10 : v; }

struct Params {
    const u16 *x, *wq, *wk, *wv, *wo, *wg, *wu, *wd, *gw;
    u16* out; char* ws;
};

DI void publish(char* fl, unsigned off, u64 tag) {
    asm volatile("s_waitcnt vmcnt(0)\n\ts_barrier" ::: "memory");
    if (threadIdx.x == 0) __hip_atomic_store((GAS u64*)(fl + off), tag, __ATOMIC_RELAXED, __HIP_MEMORY_SCOPE_AGENT);
}
template <int SLEEP = POLL_SLEEP> DI void wait_flags(const char* fl, unsigned voff, u64 tag) {
#pragma nounroll
    for (int spin = 0; spin < 100000; ++spin) {
        u64 v; asm volatile("global_load_dwordx2 %0, %1, %2 sc1\n\ts_waitcnt vmcnt(0)" : "=&v"(v) : "v"(voff), "s"(fl) : "memory");
        if (__builtin_amdgcn_ballot_w64(v != tag) == 0) break;
        __builtin_amdgcn_s_sleep(SLEEP);
    }
}

DI void kv_stage(const char* kv, int c, unsigned lds, int w, int lane);
constexpr unsigned L_Q = 2 * KV_CHUNK, QROW = 168, L_OR = 81904;
#define IC(v) std::integral_constant<int, v>{}
DI float q_phase(const Params& P, int rb, int cb, unsigned lds0, int w, int lane, u64 tag SARG) {
    const int n = lane & 15, g = lane >> 4, q = (lane >> 2) & 3, p = lane & 3;
    constexpr unsigned SL = 18432;
    const unsigned vX0 = (lane >> 3) * 1280 + (((lane & 7) ^ ((lane >> 4) & 3)) * 16) + 1024, vX1 = (vX0 ^ 64) + 8 * 1280 - 1024;
    unsigned vW[3];
#pragma unroll
    for (int j = 0; j < 3; ++j) { const int b = j * 1024 + 16 * lane, r = b / 160, ch = (b % 160) >> 4; vW[j] = krow_of((16 * (w & 1) + r) & 31) * 1280 + ch * 16 + 2048 - 1024 * j; }
    const char* baseX = (const char*)P.x + (size_t)(64 * rb + 16 * w) * 1280 - 1024;
    const char* baseW = (const char*)P.wq + (size_t)(32 * (w >> 1)) * 1280 + cb * 160 - 2048;
    const unsigned mX = lds0 + w * 2048, mW = lds0 + 8192 + w * 2560;
    int ku = (cb + 3 * (rb & 3)) % 10;
    const char *bX, *bW;
    auto nextU = [&]() { bX = baseX + ku * 128; bW = baseW + (unsigned)ku * 81920u; ku = ku == 9 ? 0 : ku + 1; };
    auto dX = [&](int u) { set_m0(mX + ((u + 2) & 3) * SL); dma<0>(bX, vX0); dma<1024>(bX, vX1); };
    auto dW0 = [&](int u) { set_m0(mW + ((u + 2) & 3) * SL); dma<0>(bW, vW[0]); };
    auto dW2 = [&]() { asm volatile("s_mov_b32 exec_hi, 0\n\tglobal_load_lds_dwordx4 %0, %1 offset:2048\n\ts_mov_b32 exec_hi, -1" ::"v"(vW[2]), "s"(bW) : "memory"); };
    asm volatile("" :: "s"(baseX), "s"(baseW)); STAMP(10);
#pragma unroll
    for (int u = 0; u < 3; ++u) { nextU(); dX(u); dW0(u); dma<1024>(bW, vW[1]); dW2(); if (u == 0) STAMP(11); if (u == 1) STAMP(12); }
    const unsigned rA = lds0 + 8192 + ((g >> 1) * 16 + (g & 1) * 4 + q) * 160 + 8 * p, rB = lds0 + (16 * w + n) * 128 + ((g ^ ((n >> 1) & 7)) * 16);
    unsigned rA3 = rA + 3 * SL, rB1 = rB ^ 64; asm volatile("" : "+v"(rA3), "+v"(rB1));
    f32x4 acc[5];
#pragma unroll
    for (int t = 0; t < 5; ++t) acc[t] = (f32x4){0, 0, 0, 0};
    bf16x8 F[2][5], X[2];
    auto rdX = [&](int u, int s) { X[s] = lds128((s ? rB1 : rB) + ((u + 2) & 3) * SL); };
    auto rdF = [&](int u, int s, int t) { const unsigned a = (((u + 2) & 3) == 3 ? rA3 : rA + ((u + 2) & 3) * SL) + s * 5120 + t * 32; F[s][t] = cat(trd(a), trd(a + 1280)); };
    const char* fl = P.ws + WS_FL; const char* kvp = P.ws + WS_KV;
    u64 f0; bool early = false;
    STAMP(8);
    wait_bar<10>();
    STAMP(9);
    rdX(0, 0); rdF(0, 0, 0); rdF(0, 0, 1); rdF(0, 0, 2); rdF(0, 0, 3); rdF(0, 0, 4);
    SBAR();
#define MM(s, t) acc[t] = mfma16(F[s][t], X[s], acc[t])
    sfor<0, 10>([&](auto U) {
        constexpr int u = U; constexpr bool ld = u + 3 < 10, nx = u + 1 < 10;
        if constexpr (ld) nextU();
        MM(0, 0); rdX(u, 1); rdF(u, 1, 0); SBAR();
        MM(0, 1); rdF(u, 1, 1); rdF(u, 1, 2); SBAR();
        MM(0, 2); rdF(u, 1, 3); rdF(u, 1, 4); SBAR();
        MM(0, 3); if constexpr (ld) dX(u + 3); SBAR();
        MM(0, 4); SBAR();
        if constexpr (u == 8) { wait_vm<0>(); asm volatile("" : "+v"(f0)); early = __builtin_amdgcn_ballot_w64(f0 != tag) == 0; }
        if constexpr (nx) { if constexpr (u <= 6) wait_bar<7>(); else if constexpr (u == 7) wait_bar<5>(); else wait_bar<0>(); } else wait_lgkm0();
        SBAR();
        MM(1, 0); if constexpr (nx) { rdX(u + 1, 0); rdF(u + 1, 0, 0); } SBAR();
        MM(1, 1); if constexpr (nx) { rdF(u + 1, 0, 1); rdF(u + 1, 0, 2); } SBAR();
        MM(1, 2); if constexpr (nx) { rdF(u + 1, 0, 3); rdF(u + 1, 0, 4); } SBAR();
        MM(1, 3); if constexpr (ld) { dW0(u + 3); dma<1024>(bW, vW[1]); } SBAR();
        MM(1, 4); if constexpr (ld) dW2(); SBAR();
        if constexpr (u == 7) asm volatile("global_load_dwordx2 %0, %1, %2 sc1" : "=v"(f0) : "v"(FL_CH + (lane & 7) * FS), "s"(fl) : "memory");
        if constexpr (u == 8) { if (early) kv_stage(kvp, 0, lds0, w, lane); }
    });
#undef MM
    STAMP(3);
    if (!early && w == 0) wait_flags(fl, FL_CH + (lane & 7) * FS, tag);
    bar();
    if (!early) kv_stage(kvp, 0, lds0, w, lane);
    float gt; asm volatile("global_load_dword %0, %1, %2 sc1" : "=v"(gt) : "v"((64 * rb + 32 * (w & 1) + (lane & 31)) * 4), "s"(P.ws + WS_G) : "memory");
    kv_stage(kvp, 1, lds0 + KV_CHUNK, w, lane);
    { const unsigned a = lds0 + L_Q + (16 * w + n) * QROW + g * 8;
#pragma unroll
      for (int t = 0; t < 5; ++t) *(LAS u32x2*)(a + 32 * t) = (u32x2){cvtpk(acc[t][0] * QSCALE, acc[t][1] * QSCALE), cvtpk(acc[t][2] * QSCALE, acc[t][3] * QSCALE)}; }
    if (threadIdx.x == 0) *(LAS unsigned*)(lds0 + L_OR) = 0u;
    STAMP(4);
    wait_bar<6>();
    asm volatile("" : "+v"(gt));
    return gt;
}

DI void kv_job(const Params& P, int tt, bool isV, unsigned lds0, int w, int lane SARG) {
    const int n = lane & 15, g = lane >> 4, q = (lane >> 2) & 3, p = lane & 3;
    const unsigned mine = lds0 + w * 12288, red = lds0 + 69632;
    { const char* bx = (const char*)P.x + (size_t)(16 * tt) * 1280 + 320 * w;
      asm volatile("" :: "s"(bx)); STAMP(10);
#pragma unroll
      for (int j = 0; j < 5; ++j) { const int c = 64 * j + lane; glds(bx, (c / 20) * 1280 + (c % 20) * 16, mine + j * 1024); }
      STAMP(11);
      const char* bw = (const char*)(isV ? P.wv : P.wk) + 6400 * w;
#pragma unroll
      for (int j = 0; j < 6; ++j) glds(bw + j * 1024, lane * 16, mine + 5120 + j * 1024);
      if (lane < 16) glds(bw + 6144, lane * 16, mine + 5120 + 6144); }
    bf16x8 gf[5];
#pragma unroll
    for (int s = 0; s < 5; ++s) asm volatile("global_load_dwordx4 %0, %1, %2" : "=v"(gf[s]) : "v"(g * 16), "s"((const char*)P.gw + 320 * w + 64 * s) : "memory");
    STAMP(12);
    wait_vm<12>();
    STAMP(13);
    wait_vm<0>();
    STAMP(14);
    asm volatile("" : "+v"(gf[0]), "+v"(gf[1]), "+v"(gf[2]), "+v"(gf[3]), "+v"(gf[4]));
    bf16x8 xb[5], a0[5], a1[5];
    const unsigned rW = mine + 5120 + (8 * g + q) * 40;
#pragma unroll
    for (int s = 0; s < 5; ++s) {
        xb[s] = lds128(mine + n * 320 + g * 16 + s * 64);
        a0[s] = cat(trd(rW + 8 * p + s * 1280), trd(rW + 8 * p + s * 1280 + 160)); a1[s] = cat(trd(rW + 32 + s * 1280), trd(rW + 32 + s * 1280 + 160));
    }
    SBAR();
    f32x4 e0 = {0, 0, 0, 0}, e1 = e0, e2 = e0;
    if (isV) {
#pragma unroll
        for (int s = 0; s < 5; ++s) { e0 = mfma16(xb[s], a0[s], e0); e1 = mfma16(xb[s], a1[s], e1); }
    } else {
#pragma unroll
        for (int s = 0; s < 5; ++s) { e0 = mfma16(a0[s], xb[s], e0); e1 = mfma16(a1[s], xb[s], e1); e2 = mfma16(gf[s], xb[s], e2); }
    }
    *(LAS f32x4*)(red + w * 1024 + lane * 16) = e0; *(LAS f32x4*)(red + 4096 + w * 1024 + lane * 16) = e1; *(LAS f32x4*)(red + 8192 + w * 1024 + lane * 16) = e2;
    bar();
    if (w < 3) {
        const unsigned r = red + w * 4096 + lane * 16;
        const f32x4 e = (*(LAS f32x4*)r + *(LAS f32x4*)(r + 1024)) + (*(LAS f32x4*)(r + 2048) + *(LAS f32x4*)(r + 3072));
        char* ck = P.ws + WS_KV + (tt >> 4) * KV_CHUNK;
        const int tl = (tt >> 1) & 7, sv = tt & 1;
        const u32x2 ev = {cvtpk(e[0], e[1]), cvtpk(e[2], e[3])};
        if (isV) {
            const unsigned vo = KV_V + tl * KV_VT + sv * 768 + (g & 1) * 8;
            if (w == 0) st8(ck, vo + (g >> 1) * 256 + n * 16, ev);
            else if (w == 1 && n < 8) st8(ck, vo + 512 + (g >> 1) * 128 + n * 16, n < 4 ? ev : (n == 4 ? (u32x2){0x3F803F80u, 0x3F803F80u} : (u32x2){0u, 0u}));
        } else {
            if (w == 0) st8(ck, tl * 1024 + (32 * (g >> 1) + 16 * sv + n) * 16 + (g & 1) * 8, ev);
            else if (w == 1) { if (g < 2) st4(ck, KV_K2 + tl * 256 + sv * 128 + g * 64 + n * 4, g ? ev[1] : ev[0]); }
            else if (g == 0) st4(P.ws + WS_G, (16 * tt + n) * 4, __float_as_uint(1.f / (1.f + __expf(-e[0]))));
        }
    }
}

DI void kv_stage(const char* kv, int c, unsigned lds, int w, int lane) {
#pragma unroll
    for (int j = 0; j < 5; ++j) glds_sc1(kv + c * KV_CHUNK + w * 5632 + j * 1024, lane * 16, lds + w * 5632 + j * 1024);
    if (lane < 32) glds_sc1(kv + c * KV_CHUNK + w * 5632 + 5120, lane * 16, lds + w * 5632 + 5120);
}
DI void attn_safe(const char* kv, unsigned lds0, int w, int lane, const bf16x8 (&q1)[2], const unsigned (&q2)[2], f32x16 (&o)[2]) {
    const int d = lane & 31, ha = lane >> 5;
    const unsigned rK = lds0 + lane * 16, rK2 = lds0 + KV_K2 + (d >> 4) * 128 + ha * 64 + (d & 15) * 4, rV = lds0 + KV_V + (d < 16 ? ha * 256 + d * 16 : 512 + ha * 128 + min(d - 16, 5) * 16);
#pragma unroll
    for (int t = 0; t < 2; ++t)
#pragma unroll
        for (int r = 0; r < 16; ++r) o[t][r] = 0.f;
    float m[2] = {-3.0e38f, -3.0e38f};
    for (int c = 0; c < 8; ++c) {
        bar();
        kv_stage(kv, c, lds0, w, lane);
        wait_bar<0>();
        for (int t = 0; t < 8; ++t) {
            const bf16x8 k1 = lds128(rK + t * 1024); const unsigned k2 = *(LAS unsigned*)(rK2 + t * 256);
            const bf16x8 v0 = lds128(rV + t * KV_VT), v1 = lds128(rV + t * KV_VT + 768);
#pragma unroll
            for (int u = 0; u < 2; ++u) {
                f32x16 s;
#pragma unroll
                for (int r = 0; r < 16; ++r) s[r] = 0.f;
                s = mfma32(k1, q1[u], s); s = mfma32k8((u32x2){k2, 0u}, (u32x2){q2[u], 0u}, s);
                float mx = s[0];
#pragma unroll
                for (int r = 1; r < 16; ++r) mx = fmaxf(mx, s[r]);
                auto rr = __builtin_amdgcn_permlane32_swap(__float_as_uint(mx), __float_as_uint(mx), false, false);
                mx = fmaxf(__uint_as_float(rr[0]), __uint_as_float(rr[1]));
                const float mn = fmaxf(m[u], mx), al = __builtin_amdgcn_exp2f(m[u] - mn); m[u] = mn;
#pragma unroll
                for (int r = 0; r < 16; ++r) { o[u][r] *= al; s[r] = __builtin_amdgcn_exp2f(s[r] - mn); }
                o[u] = smfmac(v0, __builtin_shufflevector(s, s, 0, 1, 2, 3, 4, 5, 6, 7), o[u], 0xDDDDDDDD);
                o[u] = smfmac(v1, __builtin_shufflevector(s, s, 8, 9, 10, 11, 12, 13, 14, 15), o[u], 0xDDDDDDDD);
            }
        }
    }
}
#define STR_(x) #x
#define STR(x) STR_(x)
#define R2(a) "v[" STR(a) ":" STR(a) "+1]"
#define R4(a) "v[" STR(a) ":" STR(a) "+3]"
#define R16(a) "v[" STR(a) ":" STR(a) "+15]"
#define E(r) "v_exp_f32 v[" STR(r) "], v[" STR(r) "]\n\t"
#define M32(N, K, Q) "v_mfma_f32_32x32x16_bf16 " R16(N) ", " R4(K) ", " Q ", 0\n\t"
#define M8(N, K, Q) "v_mfma_f32_32x32x8_bf16 " R16(N) ", " R2(K) ", " Q ", " R16(N) "\n\t"
#define SM(O, V, B) "v_smfmac_f32_32x32x32_bf16 " O ", " R4(V) ", v[" STR(B) ":" STR(B) "+7], %[idx]\n\t"
#define LK(d, off) "ds_read_b128 " R4(d) ", %[rK] offset:" STR(off) "\n\t"
#define LV(d, off) "ds_read_b128 " R4(d) ", %[rV] offset:" STR(off) "\n\t"
#define WL(n) "s_waitcnt lgkmcnt(" STR(n) ")\n\t"
#define SA0 192
#define SB0 208
#define SA1 224
#define SB1 240
#define K1A 160
#define K1B 164
#define K2A 168
#define K2B 170
#define V0A 176
#define V0B 180
#define V1A 184
#define V1B 188
#define STEP(N, C, Pv, K1, K2, Q1, Q2, VH, OH, VL, OL, XL, XM, X8, XH) \
    XL E(C) E(C+1) E(C+2) M32(N, K1, Q1) XM E(C+3) E(C+4) E(C+5) E(C+6) M8(N, K2, Q2) X8 E(C+7) E(C+8) E(C+9) E(C+10) SM(OH, VH, Pv+8) XH \
    E(C+11) E(C+12) E(C+13) E(C+14) E(C+15) SM(OL, VL, C)
#define LK2(d, off) "ds_read_b32 v[" STR(d) "], %[rK2] offset:" STR(off) "\n\t"
#define PAIR(VO1, VO2, T2, T3, A1M, A18, B1H, A2M, A28, A3M, A38, A4M, A48) \
    STEP(SB0, SA0, SB1, K1A, K2A, "%[q1b]", "%[q2b]", V1B, "%[o1]", V0A, "%[o0]", LV(V0B, VO1), A1M, A18, LV(V1B, VO1+768) B1H) \
    STEP(SA1, SB0, SA0, K1B, K2B, "%[q1a]", "%[q2a]", V1A, "%[o0]", V0A, "%[o1]", WL(1), LK2(K2A, T2*256) A2M, A28, LK(K1A, T2*1024)) \
    STEP(SB1, SA1, SB0, K1B, K2B, "%[q1b]", "%[q2b]", V1A, "%[o1]", V0B, "%[o0]", LV(V0A, VO2), A3M, A38, LV(V1A, VO2+768)) \
    STEP(SA0, SB1, SA1, K1A, K2A, "%[q1a]", "%[q2a]", V1B, "%[o0]", V0B, "%[o1]", WL(1), LK2(K2B, T3*256) A4M, A48, LK(K1B, T3*1024))
#define DMA(off) "global_load_lds_dwordx4 %[voff], %[kvp] offset:" STR(off) " sc1\n\t"
#define DMA2(off) "global_load_lds_dwordx4 %[voff2], %[kvp] offset:" STR(off) " sc1\n\t"
#define CL10(a) "v" #a "0", "v" #a "1", "v" #a "2", "v" #a "3", "v" #a "4", "v" #a "5", "v" #a "6", "v" #a "7", "v" #a "8", "v" #a "9"
#define KCLOB CL10(16), CL10(17), CL10(18), CL10(19), CL10(20), CL10(21), CL10(22), CL10(23), CL10(24), "v250", "v251", "v252", "v253", "v254", "v255"
#define DSTAMP
#define DOPS
#define DCLOB
#define CHUNK_HEAD \
    PAIR(1*1536, 2*1536, 2, 3, "", "", "", "", "", "", "", "", "") \
    PAIR(3*1536, 4*1536, 4, 5, "", "", "", "", "", "", "", "", "") \
    PAIR(5*1536, 6*1536, 6, 7, "", "", "", "", "", "", "", "", "") \
    DSTAMP "s_cmp_eq_u32 %[slot], 2\n\ts_cselect_b32 %[dlt], %[dneg], %[dpos]\n\ts_waitcnt vmcnt(0)\n\t" DSTAMP "s_barrier\n\t" DSTAMP \
    "v_add_u32 %[rK], %[dlt], %[rK]\n\tv_add_u32 %[rK2], %[dlt], %[rK2]\n\ts_add_u32 %[slot], %[slot], 1\n\ts_cmp_eq_u32 %[slot], 3\n\ts_cselect_b32 %[slot], 0, %[slot]\n\t"
#define VSW "v_add_u32 %[rV], %[dlt], %[rV]\n\t"
DI void attn_fast(const char* kv, unsigned lds0, int w, int lane, const bf16x8 (&q1)[2], const unsigned (&q2)[2], f32x16 (&o)[2]) {
    const int d = lane & 31, ha = lane >> 5;
    unsigned rK = lds0 + lane * 16, rK2 = lds0 + KV_K2 + (d >> 4) * 128 + ha * 64 + (d & 15) * 4, rV = lds0 + KV_V + (d < 16 ? ha * 256 + d * 16 : 512 + ha * 128 + min(d - 16, 5) * 16);
    const u32x2 q2a = {q2[0], 0u}, q2b = {q2[1], 0u};
    f32x16 o0, o1;
#pragma unroll
    for (int r = 0; r < 16; ++r) { o0[r] = 0.f; o1[r] = 0.f; }
    unsigned voff = 2 * KV_CHUNK + w * 5632 + lane * 16, voff2 = voff + 4096;
    unsigned mdst = lds0 + 2 * KV_CHUNK + w * 5632, slot = 0, cnt = 6, dlt, mlo = lds0 + w * 5632;
    asm volatile(
        LK(K1A, 0) LK2(K2A, 0) LV(V0A, 0) LV(V1A, 768) LK(K1B, 1024) LK2(K2B, 256) "v_mov_b32 v169, 0\n\tv_mov_b32 v171, 0\n\t"
        "v_mov_b32 v248, 0\n\tv_mov_b32 v249, 0\n\tv_mov_b32 v250, 0\n\tv_mov_b32 v251, 0\n\tv_mov_b32 v252, 0\n\tv_mov_b32 v253, 0\n\tv_mov_b32 v254, 0\n\tv_mov_b32 v255, 0\n\t"
        "v_mov_b32 v188, 0\n\tv_mov_b32 v189, 0\n\tv_mov_b32 v190, 0\n\tv_mov_b32 v191, 0\n\t"
        WL(0) M32(SA0, K1A, "%[q1a]") M8(SA0, K2A, "%[q2a]") "s_nop 15\n\t"
        "LA%=:\n\t"
        CHUNK_HEAD
        PAIR(7*1536, 0, 0, 1, "s_mov_b32 m0, %[mdst]\n\t", DMA(0), VSW,
             "", DMA(1024), DMA(2048), DMA(3072) "s_add_u32 m0, m0, 4096\n\t",
             DMA2(0), "s_mov_b32 exec_hi, 0\n\t" DMA2(1024) "s_mov_b32 exec_hi, -1\n\t")
        "v_add_u32 %[voff], %[dpos], %[voff]\n\tv_add_u32 %[voff2], %[dpos], %[voff2]\n\t"
        "s_add_u32 %[mdst], %[mdst], 22528\n\ts_cmp_eq_u32 %[slot], 1\n\ts_cselect_b32 %[mdst], %[mlo], %[mdst]\n\t"
        "s_sub_u32 %[cnt], %[cnt], 1\n\ts_cmp_lg_u32 %[cnt], 0\n\ts_cbranch_scc1 LA%=\n\t"
        "s_mov_b32 %[cnt], 2\n\t"
        "LB%=:\n\t"
        CHUNK_HEAD
        PAIR(7*1536, 0, 0, 1, "", "", VSW, "", "", "", "", "", "")
        "s_sub_u32 %[cnt], %[cnt], 1\n\ts_cmp_lg_u32 %[cnt], 0\n\ts_cbranch_scc1 LB%=\n\t"
        SM("%[o1]", V1B, SB1+8) "s_waitcnt lgkmcnt(0)\n\ts_nop 15\n\ts_nop 3"
        : [o0] "+v"(o0), [o1] "+v"(o1), [rK] "+v"(rK), [rK2] "+v"(rK2), [rV] "+v"(rV), [voff] "+v"(voff), [voff2] "+v"(voff2), [mdst] "+s"(mdst), [slot] "+s"(slot), [cnt] "+s"(cnt), [dlt] "=&s"(dlt) DOPS
        : [q1a] "v"(q1[0]), [q1b] "v"(q1[1]), [q2a] "v"(q2a), [q2b] "v"(q2b), [idx] "v"(0xDDDDDDDD), [kvp] "s"(kv),
          [dneg] "s"(-2 * KV_CHUNK), [dpos] "s"(KV_CHUNK), [mlo] "s"(mlo)
        : "memory", "scc", KCLOB DCLOB);
    o[0] = o0; o[1] = o1;
}

DI float upper_half(float v) { auto r = __builtin_amdgcn_permlane32_swap(__float_as_uint(v), __float_as_uint(v), false, false); return __uint_as_float(r[1]); }
DI void role_attn(const Params& P, int rb, int cb, unsigned lds0, int w, int lane, u64 tag SARG) {
    __builtin_amdgcn_s_setprio(2);
    const float gt = q_phase(P, rb, cb, lds0, w, lane, tag SPASS);
    __builtin_amdgcn_s_setprio(A_PRIO);
    const char* kv = P.ws + WS_KV;
    const int n = lane & 31, h = lane >> 5, hp = w >> 1, T = 2 * rb + (w & 1);
    bf16x8 q1[2]; unsigned q2[2];
#pragma unroll
    for (int u = 0; u < 2; ++u) {
        const unsigned a = lds0 + L_Q + (32 * (w & 1) + n) * QROW + 80 * hp + 40 * u;
        q1[u] = cat(*(LAS s16x4*)(a + 16 * h), *(LAS s16x4*)(a + 16 * h + 8)); q2[u] = *(LAS unsigned*)(a + 32 + 4 * h);
    }
    f32x16 o[2];
    STAMP(5);
    attn_fast(kv, lds0, w, lane, q1, q2, o);
    __builtin_amdgcn_s_setprio(3);
    STAMP(6);
    char* O = P.ws + WS_O + (size_t)T * 40960 + (10 * cb + 5 * hp) * 512;
    auto store_o = [&](float l0, float l1) {
        const float i0 = gt * __builtin_amdgcn_rcpf(l0), i1 = gt * __builtin_amdgcn_rcpf(l1);
        unsigned A[3][2], B[3][2];
#pragma unroll
        for (int k = 0; k < 3; ++k) {
            A[k][0] = cvtpk(o[0][4 * k] * i0, o[0][4 * k + 1] * i0); A[k][1] = cvtpk(o[0][4 * k + 2] * i0, o[0][4 * k + 3] * i0);
            B[k][0] = cvtpk(o[1][4 * k] * i1, o[1][4 * k + 1] * i1); B[k][1] = cvtpk(o[1][4 * k + 2] * i1, o[1][4 * k + 3] * i1);
        }
        { auto r0 = __builtin_amdgcn_permlane32_swap(A[0][0], A[1][0], false, false); auto r1 = __builtin_amdgcn_permlane32_swap(A[0][1], A[1][1], false, false);
          st16(O, lane * 16, (u32x4){r0[0], r1[0], r0[1], r1[1]}); }
        if (!h) st16(O, 1024 + n * 16, (u32x4){A[2][0], A[2][1], B[0][0], B[0][1]});
        { auto r0 = __builtin_amdgcn_permlane32_swap(B[0][0], B[2][0], false, false); auto r1 = __builtin_amdgcn_permlane32_swap(B[0][1], B[2][1], false, false);
          st16(O, 1536 + lane * 16, h ? (u32x4){B[1][0], B[1][1], r0[0], r1[0]} : (u32x4){r0[1], r1[1], B[1][0], B[1][1]}); }
    };
    const float l0 = upper_half(o[0][8]), l1 = upper_half(o[1][8]);
    store_o(l0, l1);
    const bool bad = !(l0 > 0x1p-80f && l0 < 0x1p80f && l1 > 0x1p-80f && l1 < 0x1p80f);
    if (__builtin_amdgcn_ballot_w64(bad) != 0 && lane == 0) *(LAS unsigned*)(lds0 + L_OR) = 1u;
    wait_bar<0>();
    if (__builtin_amdgcn_readfirstlane(*(volatile LAS unsigned*)(lds0 + L_OR))) {
        attn_safe(kv, lds0, w, lane, q1, q2, o);
        store_o(upper_half(o[0][8]), upper_half(o[1][8]));
        wait_bar<0>();
    }
    if (threadIdx.x == 0) __hip_atomic_store((GAS u64*)(P.ws + WS_FL + FL_O + (rb * 8 + cb) * FS), tag, __ATOMIC_RELAXED, __HIP_MEMORY_SCOPE_AGENT);
    STAMP(7);
}

struct OutCtx { unsigned voffW[4], ldsw; int rot; const char *wd, *wo; };
DI void out_stageW(const OutCtx& C, int st, int w) {
    const int v = st + C.rot;
    const char* b = st < 12 ? C.wd + (unsigned)(v >= 12 ? v - 12 : v) * 40960u : C.wo + (st - 12) * 40960;
    set_m0(C.ldsw + (st % 5) * 4096); dma<0>(b, C.voffW[0]); dma<1024>(b, C.voffW[1]); dma<2048>(b, C.voffW[2]); dma<3072>(b, C.voffW[3]);
}
DI void out_init(OutCtx& C, const Params& P, int tb, int cj, unsigned lds0, int w, int lane) {
#pragma unroll
    for (int j = 0; j < 4; ++j) C.voffW[j] = (8 * j + (lane >> 3)) * 1280 + (((lane & 7) ^ (((lane >> 4) & 1) << 2)) * 16) + 3072 - 1024 * j;
    C.ldsw = lds0 + w * 20480; C.rot = (cj + 4 * (tb % 3)) % 12;
    C.wd = (const char*)P.wd + (size_t)(12 * w) * 40960 + cj * 128 - 3072; C.wo = (const char*)P.wo + (size_t)(5 * w) * 40960 + cj * 128 - 3072;
}
DI float gelu_mul(float a, float u) {
    const float t = a * a, z = a * (-2.302208198f - 0.1029432397f * t);
    return a * u * __builtin_amdgcn_rcpf(1.f + __builtin_amdgcn_exp2f(z));
}
template <class F> DI void ffnup_phase(const Params& P, int RB, int CB, int rot, unsigned lds0, int w, int lane, u64 tag, F&& mid SARG) {
    constexpr int L_W = 32768, WSL = 12288;
    const int wn = w & 1, wf = w >> 1;
    unsigned voffW[3], voffX[4];
#pragma unroll
    for (int j = 0; j < 3; ++j) { const int b = ((w & 1) * 3 + j) * 1024 + 16 * lane, r = b / 192, ch = ((b % 192) >> 4) ^ ((w >> 1) ? 2 : 0); voffW[j] = r * 3072 + ch * 16 + 2048 - 1024 * j; }
#pragma unroll
    for (int j = 0; j < 4; ++j) voffX[j] = (8 * j + (lane >> 3)) * 1280 + (((lane & 7) ^ ((lane >> 4) & 3) ^ ((j & 1) * 4)) * 16) + 3072 - 1024 * j;
    const char* baseW = (const char*)((w >> 1) ? P.wu : P.wg) + CB * 192 - 2048;
    const char* baseX = (const char*)P.x + (size_t)(128 * RB + 32 * w) * 1280 - 3072;
    const unsigned mW = __builtin_amdgcn_readfirstlane(lds0 + L_W + w * 3072), mX = __builtin_amdgcn_readfirstlane(lds0 + w * 4096);
    int kW = rot * 2, kX = rot;
    auto nextW = [&]() { const char* b = baseW + (unsigned)kW * (32u * 3072u); kW = kW == 19 ? 0 : kW + 1; return b; };
    auto nextX = [&]() { const char* b = baseX + (unsigned)kX * 128u; kX = kX == 9 ? 0 : kX + 1; return b; };
    { const char* b = nextX(); set_m0(mX); SBAR(); dma<0>(b, voffX[0]); dma<1024>(b, voffX[1]); dma<2048>(b, voffX[2]); dma<3072>(b, voffX[3]); }
#pragma unroll
    for (int i = 0; i < 3; ++i) { const char* b = nextW(); set_m0(mW + i * WSL); SBAR(); dma<0>(b, voffW[0]); dma<1024>(b, voffW[1]); dma<2048>(b, voffW[2]); }
    const char* bX = nextX();
    set_m0(mX + 16384); SBAR(); dma<0>(bX, voffX[0]); dma<1024>(bX, voffX[1]);
    const int h = lane >> 5, g = (lane >> 4) & 1, q = (lane >> 2) & 3, p = lane & 3, n = lane & 31;
    unsigned rB[4], rAw[3];
#pragma unroll
    for (int s = 0; s < 4; ++s) rB[s] = lds0 + (64 * wn + n) * 128 + (((2 * s + h) ^ ((n >> 1) & 7)) * 16);
#pragma unroll
    for (int a = 0; a < 3; ++a) { const int T = 3 * wf + a; rAw[a] = lds0 + L_W + g * 6144 + (8 * h + q) * 192 + (((T & 1) ^ g) * 32) + 8 * p + (T >> 1) * 64; }
    f32x16 acc[3][2];
#pragma unroll
    for (int a = 0; a < 3; ++a)
#pragma unroll
        for (int b = 0; b < 2; ++b)
#pragma unroll
            for (int r = 0; r < 16; ++r) acc[a][b][r] = 0.f;
    bf16x8 A0[3], B0[2], A1[3], B1[2];
    auto rdB = [&](bf16x8 (&B)[2], int i, int s) {
#pragma unroll
        for (int b = 0; b < 2; ++b) B[b] = lds128(rB[2 * (i & 1) + s] + ((i >> 1) & 1) * 16384 + b * 4096); };
    auto rdA = [&](bf16x8 (&A)[3], int a, int i, int s) { const unsigned ad = rAw[a] + (i & 3) * WSL + 16 * s * 192; A[a] = cat(trd(ad), trd(ad + 768)); };
    wait_bar<8>();
    rdB(B0, 0, 0); rdA(A0, 0, 0, 0); rdA(A0, 1, 0, 0); rdA(A0, 2, 0, 0);
    SBAR();
#define MM(A, B, a, b) acc[a][b] = mfma32(A[a], B[b], acc[a][b])
    auto stagef = [&](auto I, auto LW, auto LX, auto NV, auto NXT) {
        constexpr int i = I; constexpr bool lw = LW, lx = LX, nx = NXT; constexpr int nv = NV;
        const char* bW = nullptr;
        MM(A0, B0, 0, 0); rdB(B1, i, 1); SBAR();
        MM(A0, B0, 0, 1); rdA(A1, 0, i, 1); SBAR();
        MM(A0, B0, 1, 0); rdA(A1, 1, i, 1); if constexpr (lx && !(i & 1)) { set_m0(mX + (((i >> 1) + 1) & 1) * 16384); } SBAR();
        MM(A0, B0, 1, 1); rdA(A1, 2, i, 1); if constexpr (lx && !(i & 1)) { dma<2048>(bX, voffX[2]); dma<3072>(bX, voffX[3]); } if constexpr (lw) { bW = nextW(); set_m0(mW + ((i + 3) & 3) * WSL); } SBAR();
        MM(A0, B0, 2, 0); if constexpr (lw) dma<0>(bW, voffW[0]); SBAR();
        MM(A0, B0, 2, 1); if constexpr (lw) dma<1024>(bW, voffW[1]); SBAR();
        if constexpr (nx) wait_bar<nv>(); else wait_lgkm0();
        SBAR();
        MM(A1, B1, 0, 0); if constexpr (nx) rdB(B0, i + 1, 0); SBAR();
        MM(A1, B1, 0, 1); if constexpr (nx) rdA(A0, 0, i + 1, 0); SBAR();
        MM(A1, B1, 1, 0); if constexpr (nx) rdA(A0, 1, i + 1, 0); if constexpr (lw) dma<2048>(bW, voffW[2]); SBAR();
        MM(A1, B1, 1, 1); if constexpr (nx) rdA(A0, 2, i + 1, 0); if constexpr (lx && (i & 1)) { bX = nextX(); set_m0(mX + (((i >> 1) + 2) & 1) * 16384); } SBAR();
        MM(A1, B1, 2, 0); if constexpr (lx && (i & 1)) dma<0>(bX, voffX[0]); SBAR();
        MM(A1, B1, 2, 1); if constexpr (lx && (i & 1)) dma<1024>(bX, voffX[1]); SBAR();
    };
    using T_ = std::true_type; using F_ = std::false_type;
#pragma nounroll
    for (int it = 0; it < 4; ++it) {
        stagef(IC(0), T_{}, T_{}, IC(9), T_{}); stagef(IC(1), T_{}, T_{}, IC(5), T_{}); stagef(IC(2), T_{}, T_{}, IC(9), T_{}); stagef(IC(3), T_{}, T_{}, IC(5), T_{});
    }
    stagef(IC(0), T_{}, T_{}, IC(9), T_{}); stagef(IC(1), F_{}, F_{}, IC(3), T_{}); stagef(IC(2), F_{}, F_{}, IC(0), T_{}); stagef(IC(3), F_{}, F_{}, IC(0), F_{});
#undef MM
    STAMP(2);
    bar(); __builtin_amdgcn_s_setprio(0); mid(); __builtin_amdgcn_s_setprio(3);
#pragma unroll
    for (int a = 0; a < 3; ++a)
#pragma unroll
        for (int b = 0; b < 2; ++b) {
            const f32x16& c = acc[a][b];
            unsigned a0 = cvtpk(gelu_mul(c[0], c[8]), gelu_mul(c[1], c[9])), a1 = cvtpk(gelu_mul(c[2], c[10]), gelu_mul(c[3], c[11]));
            unsigned b0 = cvtpk(gelu_mul(c[4], c[12]), gelu_mul(c[5], c[13])), b1 = cvtpk(gelu_mul(c[6], c[14]), gelu_mul(c[7], c[15]));
            auto r0 = __builtin_amdgcn_permlane32_swap(a0, b0, false, false);
            auto r1 = __builtin_amdgcn_permlane32_swap(a1, b1, false, false);
            st16(P.ws + WS_H + (size_t)(4 * RB + 2 * wn + b) * 98304 + (6 * CB + 3 * wf + a) * 1024, lane * 16, (u32x4){r0[0], r1[0], r0[1], r1[1]});
        }
    publish(P.ws + WS_FL, FL_H + (RB * 16 + CB) * FS, tag);
}

DI void out_phase(const Params& P, const OutCtx& C, int tb, int cj, unsigned lds0, int w, int lane, u64 tag SARG) {
    const int n = lane & 31, h = lane >> 5, mh = (lane >> 4) & 1, q = (lane >> 2) & 3, p = lane & 3;
    const unsigned ldsw = C.ldsw;
    const char* fl = P.ws + WS_FL;
    unsigned vH[3], vO[3];
#pragma unroll
    for (int t = 0; t < 3; ++t) { const int T = min(3 * tb + t, 63); vH[t] = T * 98304 + lane * 16; vO[t] = T * 40960 + lane * 16; }
#define LDB(dst, base, vo, off) asm volatile("global_load_dwordx4 %0, %1, %2 offset:" #off " sc1" : "=v"(dst) : "v"(vo), "s"(base) : "memory")
#define PIN6(b) asm volatile("" : "+v"(b[0]), "+v"(b[1]), "+v"(b[2]), "+v"(b[3]), "+v"(b[4]), "+v"(b[5]))
    unsigned rA[2];
#pragma unroll
    for (int t = 0; t < 2; ++t) rA[t] = ldsw + (8 * h + q) * 128 + ((t ^ (q >> 1)) * 64) + mh * 32 + 8 * p;
    f32x16 acc[2][3];
#pragma unroll
    for (int a = 0; a < 2; ++a)
#pragma unroll
        for (int b = 0; b < 3; ++b)
#pragma unroll
            for (int r = 0; r < 16; ++r) acc[a][b][r] = 0.f;
    bf16x8 bf[3][6], af[2][4];
    auto rdA = [&](bf16x8 (&A)[4], int f, int slot) { const unsigned a = rA[f & 1] + slot * 4096 + (f >> 1) * 2048; A[f] = cat(trd(a), trd(a + 512)); };
    const char* Hb = P.ws + WS_H + 12 * w * 2048;
    auto hbase = [&](int st) { const int v = st + C.rot; return Hb + (unsigned)(v >= 12 ? v - 12 : v) * 2048u; };
    auto loadB = [&](const char* base, const unsigned (&vo)[3], bf16x8 (&b)[6]) { LDB(b[0], base, vo[0], 0); LDB(b[1], base, vo[1], 0); LDB(b[2], base, vo[2], 0); LDB(b[3], base, vo[0], 1024); LDB(b[4], base, vo[1], 1024); LDB(b[5], base, vo[2], 1024); };
    __builtin_amdgcn_s_setprio(0);
    { const int r0 = (96 * tb) >> 7, r1 = min((96 * tb + 95) >> 7, 15); wait_flags(fl, FL_H + (((lane & 4) ? r1 : r0) * 16 + 4 * w + (lane & 3)) * FS, tag); }
    __builtin_amdgcn_s_setprio(G_PRIO);
    STAMP(4);
    loadB(hbase(0), vH, bf[0]); loadB(hbase(1), vH, bf[1]);
    rdA(af[0], 0, 0); rdA(af[0], 1, 0); rdA(af[0], 2, 0); rdA(af[0], 3, 0);
    wait_vm<6>();
#define MM(f, tt) acc[(f) & 1][tt] = mfma32(A[f], b[3 * ((f) >> 1) + tt], acc[(f) & 1][tt])
    auto step = [&](auto ST, auto LB, auto LW, auto RN, const char* nb, const unsigned (&vo)[3], auto BI) {
        constexpr int st = ST, bi = BI; constexpr bool lb = LB, lw = LW, rn = RN;
        bf16x8(&b)[6] = bf[st % 3]; bf16x8(&A)[4] = af[st & 1]; bf16x8(&An)[4] = af[(st + 1) & 1]; bf16x8(&bn)[6] = bf[bi];
        PIN6(b); SBAR();
        MM(0, 0); if constexpr (lb) LDB(bn[0], nb, vo[0], 0); SBAR();
        MM(0, 1); if constexpr (rn) rdA(An, 0, (st + 1) % 5); SBAR();
        MM(0, 2); if constexpr (lb) LDB(bn[1], nb, vo[1], 0); SBAR();
        MM(1, 0); if constexpr (rn) rdA(An, 1, (st + 1) % 5); SBAR();
        MM(1, 1); if constexpr (lb) LDB(bn[2], nb, vo[2], 0); SBAR();
        MM(1, 2); if constexpr (rn) rdA(An, 2, (st + 1) % 5); SBAR();
        MM(2, 0); if constexpr (lb) LDB(bn[3], nb, vo[0], 1024); SBAR();
        MM(2, 1); if constexpr (rn) rdA(An, 3, (st + 1) % 5); if constexpr (lb) LDB(bn[4], nb, vo[1], 1024); SBAR();
        const char* wb = nullptr;
        MM(2, 2); if constexpr (lb) LDB(bn[5], nb, vo[2], 1024);
        if constexpr (lw) { const int v = st + 5 + C.rot; wb = st + 5 < 12 ? C.wd + (unsigned)(v >= 12 ? v - 12 : v) * 40960u : C.wo + (st + 5 - 12) * 40960; set_m0(ldsw + (st % 5) * 4096); dma<0>(wb, C.voffW[0]); } SBAR();
        MM(3, 0); if constexpr (lw) dma<1024>(wb, C.voffW[1]); SBAR();
        MM(3, 1); if constexpr (lw) dma<2048>(wb, C.voffW[2]); SBAR();
        MM(3, 2); if constexpr (lw) dma<3072>(wb, C.voffW[3]); SBAR();
    };
    sfor<0, 12>([&](auto ST) {
        constexpr int st = ST;
        step(ST, std::integral_constant<bool, (st + 2 < 12)>{}, std::true_type{}, std::true_type{}, hbase(st + 2 < 12 ? st + 2 : 0), vH, IC((st + 2) % 3));
        if constexpr (st == 0) wait_vm<10>(); else if constexpr (st + 2 < 12) wait_vm<14>(); else if constexpr (st == 10) wait_vm<8>();
    });
    STAMP(5);
    float gs[3];
    __builtin_amdgcn_s_setprio(0);
    { const int r0 = (96 * tb) >> 6, r1 = min((96 * tb + 95) >> 6, 31), r = (lane & 2) ? r1 : r0;
      wait_flags(fl, FL_CH + (lane & 7) * FS, tag);
#pragma unroll
      for (int t = 0; t < 3; ++t) gs[t] = 1.f - ld4_sc1(P.ws + WS_G, (min(3 * tb + t, 63) * 32 + n) * 4);
#pragma unroll
      for (int a = 0; a < 2; ++a)
#pragma unroll
          for (int b = 0; b < 3; ++b) acc[a][b] *= gs[b];
      wait_flags(fl, FL_O + (r * 8 + 2 * w + (lane & 1)) * FS, tag);
    }
    __builtin_amdgcn_s_setprio(3);
    STAMP(6);
    const char* Ob = P.ws + WS_O + 5 * w * 2048;
    loadB(Ob, vO, bf[0]); loadB(Ob + 2048, vO, bf[1]); loadB(Ob + 4096, vO, bf[2]);
    using T_ = std::true_type; using F_ = std::false_type;
    wait_vm<12>(); step(IC(12), F_{}, F_{}, T_{}, Ob, vO, IC(0));
    loadB(Ob + 3 * 2048, vO, bf[0]);
    wait_vm<12>(); step(IC(13), F_{}, F_{}, T_{}, Ob, vO, IC(0));
    loadB(Ob + 4 * 2048, vO, bf[1]);
    wait_vm<12>(); step(IC(14), F_{}, F_{}, T_{}, Ob, vO, IC(0));
    wait_vm<6>(); step(IC(15), F_{}, F_{}, T_{}, Ob, vO, IC(0));
    wait_vm<0>(); step(IC(16), F_{}, F_{}, F_{}, Ob, vO, IC(0));
#undef MM
    STAMP(7);
    bar();
    sfor<0, 4>([&](auto V) {
        constexpr int v = V;
        if (w != v) {
            const unsigned dst = lds0 + v * 20480 + (w > v ? w - 1 : w) * 6144 + lane * 16;
#pragma unroll
            for (int a = 0; a < 2; ++a)
#pragma unroll
                for (int b = 0; b < 3; ++b) *(LAS f32x4*)(dst + (a * 3 + b) * 1024) = (f32x4){acc[a][b][4 * v], acc[a][b][4 * v + 1], acc[a][b][4 * v + 2], acc[a][b][4 * v + 3]};
        }
    });
    f32x4 res[2][3];
    sfor<0, 4>([&](auto V) {
        constexpr int v = V;
        if (w == v) {
#pragma unroll
            for (int a = 0; a < 2; ++a)
#pragma unroll
                for (int b = 0; b < 3; ++b) res[a][b] = (f32x4){acc[a][b][4 * v], acc[a][b][4 * v + 1], acc[a][b][4 * v + 2], acc[a][b][4 * v + 3]};
        }
    });
    bar();
#pragma unroll
    for (int a = 0; a < 2; ++a)
#pragma unroll
        for (int b = 0; b < 3; ++b)
#pragma unroll
            for (int k = 0; k < 3; ++k) res[a][b] += *(LAS f32x4*)(ldsw + k * 6144 + (a * 3 + b) * 1024 + lane * 16);
#pragma unroll
    for (int b = 0; b < 3; ++b) {
        auto r0 = __builtin_amdgcn_permlane32_swap(cvtpk(res[0][b][0], res[0][b][1]), cvtpk(res[1][b][0], res[1][b][1]), false, false);
        auto r1 = __builtin_amdgcn_permlane32_swap(cvtpk(res[0][b][2], res[0][b][3]), cvtpk(res[1][b][2], res[1][b][3]), false, false);
        if (3 * tb + b < 64) *(GAS u32x4*)(P.out + (size_t)(32 * (3 * tb + b) + n) * D_ + 64 * cj + 32 * h + 8 * w) = (u32x4){r0[0], r1[0], r0[1], r1[1]};
    }
}

__global__ __launch_bounds__(256, 2) void fused_kernel(Params P) {
    extern __shared__ __attribute__((aligned(16))) char lds[];
    const unsigned lds0 = (unsigned)(uintptr_t)(LAS char*)lds;
    const int lane = threadIdx.x & 63, w = __builtin_amdgcn_readfirstlane(threadIdx.x >> 6);
    const int b = blockIdx.x & 255, xc = b & 7, idx = b >> 3;
    STAMP(0);
    const u64 tag = *(const GAS u64*)(P.ws + WS_FL) * 6364136223846793005ull + 1442695040888963407ull;
    if (blockIdx.x < 256) role_attn(P, 4 * xc + (idx >> 3), idx & 7, lds0, w, lane, tag SPASS);
    else {
        __builtin_amdgcn_s_setprio(3);
        const u64 t0 = __builtin_amdgcn_s_memrealtime();
        kv_job(P, 16 * xc + (idx >> 1), idx & 1, lds0, w, lane SPASS);
        STAMP(8);
        publish(P.ws + WS_FL, FL_KV + (32 * xc + idx) * FS, tag);
        if (idx == 0 && w == 0) {
            wait_flags<1>(P.ws + WS_FL, FL_KV + (32 * xc + (lane & 31)) * FS, tag);
            if (lane == 0) __hip_atomic_store((GAS u64*)(P.ws + WS_FL + FL_CH + xc * FS), tag, __ATOMIC_RELAXED, __HIP_MEMORY_SCOPE_AGENT);
        }
        __builtin_amdgcn_s_setprio(UP_PRIO);
        STAMP(9);
        if (G_DELAY) { while (__builtin_amdgcn_s_memrealtime() - t0 < G_DELAY) __builtin_amdgcn_s_sleep(2); }
        STAMP(1);
        const int l0 = (220 * xc) >> 3, l1 = (220 * (xc + 1)) >> 3, L = l0 + idx;
        OutCtx C;
        ffnup_phase(P, 8 * (xc >> 2) + (idx >> 2), 4 * (xc & 3) + (idx & 3), ((idx >> 2) + 3 * (idx & 3)) % 10, lds0, w, lane, tag,
                    [&]() { out_init(C, P, L / 10, L % 10, lds0, w, lane); if (L < l1) { out_stageW(C, 0, w); out_stageW(C, 1, w); out_stageW(C, 2, w); out_stageW(C, 3, w); out_stageW(C, 4, w); } } SPASS);
        STAMP(3);
        if (L < l1) out_phase(P, C, L / 10, L % 10, lds0, w, lane, tag SPASS);
        else if (b == 255 && w == 0) {
            __builtin_amdgcn_s_setprio(0);
#pragma nounroll
            for (int i = 0; i < 4; ++i) { wait_flags<32>(P.ws + WS_FL, FL_H + (i * 64 + lane) * FS, tag);
 wait_flags<32>(P.ws + WS_FL, FL_O + (i * 64 + lane) * FS, tag);
 }
            if (lane == 0) __hip_atomic_store((GAS u64*)(P.ws + WS_FL), tag, __ATOMIC_RELAXED, __HIP_MEMORY_SCOPE_AGENT);
        }
    }
}

extern "C" void kernel_launch(void* const* d_in, const int* in_sizes, int n_in, void* d_out, int out_size, void* d_ws, size_t ws_size, hipStream_t stream) {
    Params P;
    P.x = (const u16*)d_in[0]; P.wq = (const u16*)d_in[1]; P.wk = (const u16*)d_in[2]; P.wv = (const u16*)d_in[3]; P.wo = (const u16*)d_in[4];
    P.wg = (const u16*)d_in[6]; P.wu = (const u16*)d_in[7]; P.wd = (const u16*)d_in[8]; P.gw = (const u16*)d_in[9];
    P.out = (u16*)d_out; P.ws = (char*)d_ws;
    (void)hipFuncSetAttribute((const void*)fused_kernel, hipFuncAttributeMaxDynamicSharedMemorySize, LDS_BYTES);
    fused_kernel<<<512, 256, LDS_BYTES, stream>>>(P);
}
```

```cpp
#include <hip/hip_runtime.h>
#include <hip/hip_bf16.h>
#include <stdint.h>
#include <type_traits>

typedef unsigned short u16;
typedef unsigned long long u64;
typedef __attribute__((ext_vector_type(8))) __bf16 bf16x8;
typedef __attribute__((ext_vector_type(16))) __bf16 bf16x16;
typedef __attribute__((ext_vector_type(4))) short s16x4;
typedef __attribute__((ext_vector_type(4))) float f32x4;
typedef __attribute__((ext_vector_type(8))) float f32x8;
typedef __attribute__((ext_vector_type(16))) float f32x16;
typedef __attribute__((ext_vector_type(2))) unsigned u32x2;
typedef __attribute__((ext_vector_type(4))) unsigned u32x4;
#define LAS __attribute__((address_space(3)))
#define GAS __attribute__((address_space(1)))
#define DI __device__ __forceinline__

constexpr int D_ = 640;
constexpr int KV_CHUNK = 22528, KV_K2 = 8192, KV_V = 10240, KV_VT = 1536;
constexpr size_t WS_KV = 0, WS_G = 0x30000, WS_FL = 0x40000, WS_O = 1u << 20, WS_H = WS_O + 64 * 40960;
constexpr int FS = 256, FL_CH = 1024, FL_KV = 4096, FL_H = FL_KV + 256 * FS, FL_O = FL_H + 256 * FS;
constexpr float QSCALE = 0.22360679774997896f * 1.4426950408889634f;
constexpr int LDS_BYTES = 81920;
#define G_PRIO 0
#define UP_PRIO 0
#define A_PRIO 0
#define POLL_SLEEP 4
#define D_BOOST 1
#define G_DELAY 0

#define STAMP(k)
#define SARG
#define SPASS

template <int N> DI void wait_vm() { asm volatile("s_waitcnt vmcnt(%0)" ::"n"(N) : "memory"); }
DI void wait_lgkm0() { asm volatile("s_waitcnt lgkmcnt(0)" ::: "memory"); }
template <int N> DI void wait_bar() { asm volatile("s_waitcnt vmcnt(%0) lgkmcnt(0)\n\ts_barrier" ::"n"(N) : "memory"); }
DI void bar() { asm volatile("s_waitcnt lgkmcnt(0)\n\ts_barrier" ::: "memory"); }
DI void glds(const void* sbase, unsigned voff, unsigned lds) {
    asm volatile("s_mov_b32 m0, %2\n\ts_nop 0\n\tglobal_load_lds_dwordx4 %0, %1" ::"v"(voff), "s"(sbase), "s"(lds) : "memory");
}
DI void glds_sc1(const void* sbase, unsigned voff, unsigned lds) {
    asm volatile("s_mov_b32 m0, %2\n\ts_nop 0\n\tglobal_load_lds_dwordx4 %0, %1 sc1" ::"v"(voff), "s"(sbase), "s"(lds) : "memory");
}
DI void set_m0(unsigned lds) { asm volatile("s_mov_b32 m0, %0\n\ts_nop 0" ::"s"(lds) : "memory"); }
template <int OFF, int LO, int HIP = 1> DI void dmap(const void* sbase, unsigned voff) { asm volatile("s_setprio %4\n\tglobal_load_lds_dwordx4 %0, %1 offset:%2\n\ts_setprio %3" ::"v"(voff), "s"(sbase), "n"(OFF), "n"(LO), "n"(HIP) : "memory"); }
template <int OFF> DI void dma(const void* sbase, unsigned voff) { asm volatile("global_load_lds_dwordx4 %0, %1 offset:%2" ::"v"(voff), "s"(sbase), "n"(OFF) : "memory"); }
#define SBAR() __builtin_amdgcn_sched_barrier(0)
#define HI() __builtin_amdgcn_s_setprio(2)
DI void st16(char* sbase, unsigned voff, u32x4 v) { asm volatile("global_store_dwordx4 %0, %1, %2 sc1\n\ts_nop 1" ::"v"(voff), "v"(v), "s"(sbase) : "memory"); }
DI void st8(char* sbase, unsigned voff, u32x2 v) { asm volatile("global_store_dwordx2 %0, %1, %2 sc1" ::"v"(voff), "v"(v), "s"(sbase) : "memory"); }
DI void st4(char* sbase, unsigned voff, unsigned v) { asm volatile("global_store_dword %0, %1, %2 sc1" ::"v"(voff), "v"(v), "s"(sbase) : "memory"); }
DI float ld4_sc1(const char* sbase, unsigned voff) { float r; asm volatile("global_load_dword %0, %1, %2 sc1\n\ts_waitcnt vmcnt(0)" : "=&v"(r) : "v"(voff), "s"(sbase) : "memory"); return r; }
template <int OFF> DI void ldb(bf16x8& d, const char* sbase, unsigned voff) { asm volatile("global_load_dwordx4 %0, %1, %2 offset:%3 sc1" : "=v"(d) : "v"(voff), "s"(sbase), "n"(OFF) : "memory"); }
DI void pin2(bf16x8& a, bf16x8& b) { asm volatile("" : "+v"(a), "+v"(b)); }
DI unsigned cvtpk(float lo, float hi) { unsigned r; asm("v_cvt_pk_bf16_f32 %0, %1, %2" : "=v"(r) : "v"(lo), "v"(hi)); return r; }
DI s16x4 trd(unsigned a) { return __builtin_amdgcn_ds_read_tr16_b64_v4i16((LAS s16x4*)a); }
DI bf16x8 cat(s16x4 a, s16x4 b) { return __builtin_bit_cast(bf16x8, __builtin_shufflevector(a, b, 0, 1, 2, 3, 4, 5, 6, 7)); }
DI bf16x8 lds128(unsigned a) { return *(LAS bf16x8*)a; }
DI f32x4 mfma16(bf16x8 a, bf16x8 b, f32x4 c) { return __builtin_amdgcn_mfma_f32_16x16x32_bf16(a, b, c, 0, 0, 0); }
DI f32x16 mfma32(bf16x8 a, bf16x8 b, f32x16 c) { return __builtin_amdgcn_mfma_f32_32x32x16_bf16(a, b, c, 0, 0, 0); }
DI f32x16 mfma32k8(u32x2 a, u32x2 b, f32x16 c) { return __builtin_amdgcn_mfma_f32_32x32x8bf16_1k(__builtin_bit_cast(s16x4, a), __builtin_bit_cast(s16x4, b), c, 0, 0, 0); }
DI f32x16 smfmac(bf16x8 a, f32x8 b, f32x16 c, int idx) { return __builtin_amdgcn_smfmac_f32_32x32x32_bf16(a, __builtin_bit_cast(bf16x16, b), c, idx, 0, 0); }
template <int I, int N, class F> DI void sfor(F&& f) { if constexpr (I < N) { f(std::integral_constant<int, I>{}); sfor<I + 1, N>(f); } }
DI int krow_of(int rho) { const int g = ((rho >> 4) << 1) | ((rho >> 2) & 1); return 8 * g + 4 * ((rho >> 3) & 1) + (rho & 3); }
DI int mod10(int v) { return v >= 10 ? v - 10 : v; }

struct Tag { u64 e; DI void issue(const char* fl) { asm volatile("s_load_dwordx2 %0, %1, 0x0" : "=s"(e) : "s"(fl) : "memory"); }
    DI u64 get() { asm volatile("s_waitcnt lgkmcnt(0)" : "+s"(e) :: "memory"); return e * 6364136223846793005ull + 1442695040888963407ull; } };
struct Params {
    const u16 *x, *wq, *wk, *wv, *wo, *wg, *wu, *wd, *gw;
    u16* out; char* ws;
};

DI void publish(char* fl, unsigned off, u64 tag) {
    asm volatile("s_waitcnt vmcnt(0)\n\ts_barrier" ::: "memory");
    if (threadIdx.x == 0) __hip_atomic_store((GAS u64*)(fl + off), tag, __ATOMIC_RELAXED, __HIP_MEMORY_SCOPE_AGENT);
}
template <int SLEEP = POLL_SLEEP> DI void wait_flags(const char* fl, unsigned voff, u64 tag) {
#pragma nounroll
    for (int spin = 0; spin < 100000; ++spin) {
        u64 v; asm volatile("global_load_dwordx2 %0, %1, %2 sc1\n\ts_waitcnt vmcnt(0)" : "=&v"(v) : "v"(voff), "s"(fl) : "memory");
        if (__builtin_amdgcn_ballot_w64(v != tag) == 0) break;
        __builtin_amdgcn_s_sleep(SLEEP);
    }
}

DI void kv_stage(const char* kv, int c, unsigned lds, int w, int lane);
constexpr unsigned L_Q = 2 * KV_CHUNK, QROW = 168;
#define IC(v) std::integral_constant<int, v>{}
DI float q_phase(const Params& P, int rb, int cb, unsigned lds0, int w, int lane, Tag& TG, u64& tag, u64& f0 SARG) {
    const int xc = rb >> 2;
    const int n = lane & 15, g = lane >> 4, q = (lane >> 2) & 3, p = lane & 3;
    constexpr unsigned SL = 18432;
    const unsigned vX0 = (lane >> 3) * 1280 + (((lane & 7) ^ ((lane >> 4) & 3)) * 16) + 1024, vX1 = (vX0 ^ 64) + 8 * 1280 - 1024;
    unsigned vW[3];
#pragma unroll
    for (int j = 0; j < 3; ++j) { const int b = j * 1024 + 16 * lane, r = b / 160, ch = (b % 160) >> 4; vW[j] = krow_of((16 * (w & 1) + r) & 31) * 1280 + ch * 16 + 2048 - 1024 * j; }
    const char* baseX = (const char*)P.x + (size_t)(64 * rb + 16 * w) * 1280 - 1024;
    const char* baseW = (const char*)P.wq + (size_t)(32 * (w >> 1)) * 1280 + cb * 160 - 2048;
    const unsigned mX = lds0 + w * 2048, mW = lds0 + 8192 + w * 2560;
    int ku = (cb + 3 * (rb & 3)) % 10;
    const char *bX, *bW;
    auto nextU = [&]() { bX = baseX + ku * 128; bW = baseW + (unsigned)ku * 81920u; ku = ku == 9 ? 0 : ku + 1; };
    auto dX = [&](int u) { set_m0(mX + ((u + 2) & 3) * SL); dma<0>(bX, vX0); dma<1024>(bX, vX1); };
    auto dW0 = [&](int u) { set_m0(mW + ((u + 2) & 3) * SL); dma<0>(bW, vW[0]); };
    auto dW2 = [&]() { asm volatile("s_mov_b32 exec_hi, 0\n\tglobal_load_lds_dwordx4 %0, %1 offset:2048\n\ts_mov_b32 exec_hi, -1" ::"v"(vW[2]), "s"(bW) : "memory"); };
    STAMP(10);
#pragma unroll
    for (int u = 0; u < 3; ++u) { nextU(); dX(u); dW0(u); dma<1024>(bW, vW[1]); dW2(); if (u == 0) STAMP(11); }
    const unsigned rA = lds0 + 8192 + ((g >> 1) * 16 + (g & 1) * 4 + q) * 160 + 8 * p, rB = lds0 + (16 * w + n) * 128 + ((g ^ ((n >> 1) & 7)) * 16);
    unsigned rA3 = rA + 3 * SL, rB1 = rB ^ 64; asm volatile("" : "+v"(rA3), "+v"(rB1));
    f32x4 acc[5];
#pragma unroll
    for (int t = 0; t < 5; ++t) acc[t] = (f32x4){0, 0, 0, 0};
    bf16x8 F[2][5], X[2];
    auto rdX = [&](int u, int s) { X[s] = lds128((s ? rB1 : rB) + ((u + 2) & 3) * SL); };
    auto rdF = [&](int u, int s, int t) { const unsigned a = (((u + 2) & 3) == 3 ? rA3 : rA + ((u + 2) & 3) * SL) + s * 5120 + t * 32; F[s][t] = cat(trd(a), trd(a + 1280)); };
    const char* fl = P.ws + WS_FL; const char* kvp = P.ws + WS_KV;
    bool early = false;
    const unsigned vF = FL_CH + (((lane & 7) + xc) & 7) * FS;
    STAMP(8);
    wait_bar<10>();
    tag = TG.get();
    STAMP(9);
    rdX(0, 0); rdF(0, 0, 0); rdF(0, 0, 1); rdF(0, 0, 2); rdF(0, 0, 3); rdF(0, 0, 4);
    SBAR();
#define MM(s, t) acc[t] = mfma16(F[s][t], X[s], acc[t])
    sfor<0, 10>([&](auto U) {
        constexpr int u = U; constexpr bool ld = u + 3 < 10, nx = u + 1 < 10;
        if constexpr (ld) nextU();
        MM(0, 0); rdX(u, 1); rdF(u, 1, 0); SBAR();
        MM(0, 1); rdF(u, 1, 1); rdF(u, 1, 2); SBAR();
        MM(0, 2); rdF(u, 1, 3); rdF(u, 1, 4); SBAR();
        MM(0, 3); if constexpr (ld) dX(u + 3); SBAR();
        MM(0, 4); SBAR();
        if constexpr (u == 8) { wait_vm<0>(); asm volatile("" : "+v"(f0)); early = (__builtin_amdgcn_ballot_w64(f0 != tag) & 3) == 0; }
        if constexpr (nx) { if constexpr (u <= 6) wait_bar<7>(); else if constexpr (u == 7) wait_bar<5>(); else wait_bar<0>(); } else wait_lgkm0();
        SBAR();
        MM(1, 0); if constexpr (nx) { rdX(u + 1, 0); rdF(u + 1, 0, 0); } SBAR();
        MM(1, 1); if constexpr (nx) { rdF(u + 1, 0, 1); rdF(u + 1, 0, 2); } SBAR();
        MM(1, 2); if constexpr (nx) { rdF(u + 1, 0, 3); rdF(u + 1, 0, 4); } SBAR();
        MM(1, 3); if constexpr (ld) { dW0(u + 3); dma<1024>(bW, vW[1]); } SBAR();
        MM(1, 4); if constexpr (ld) dW2(); SBAR();
        if constexpr (u == 7) asm volatile("global_load_dwordx2 %0, %1, %2 sc1" : "=v"(f0) : "v"(vF), "s"(fl) : "memory");
        if constexpr (u == 8) { if (early) kv_stage(kvp, xc, lds0, w, lane); }
    });
#undef MM
    STAMP(3);
    if (!early) { wait_flags<2>(fl, FL_CH + (((lane & 1) + xc) & 7) * FS, tag); kv_stage(kvp, xc, lds0, w, lane); }
    bar();
    float gt; asm volatile("global_load_dword %0, %1, %2 sc1" : "=v"(gt) : "v"((64 * rb + 32 * (w & 1) + (lane & 31)) * 4), "s"(P.ws + WS_G) : "memory");
    kv_stage(kvp, (xc + 1) & 7, lds0 + KV_CHUNK, w, lane);
    { const unsigned a = lds0 + L_Q + (16 * w + n) * QROW + g * 8;
#pragma unroll
      for (int t = 0; t < 5; ++t) *(LAS u32x2*)(a + 32 * t) = (u32x2){cvtpk(acc[t][0] * QSCALE, acc[t][1] * QSCALE), cvtpk(acc[t][2] * QSCALE, acc[t][3] * QSCALE)}; }
    STAMP(4);
    wait_bar<6>();
    asm volatile("" : "+v"(gt));
    return gt;
}

DI void kv_job(const Params& P, int tt, bool isV, unsigned lds0, int w, int lane SARG) {
    const int n = lane & 15, g = lane >> 4, q = (lane >> 2) & 3, p = lane & 3;
    const unsigned mine = lds0 + w * 12288, red = lds0 + 69632;
    { const char* bx = (const char*)P.x + (size_t)(16 * tt) * 1280 + 320 * w;
      STAMP(10);
#pragma unroll
      for (int j = 0; j < 5; ++j) { const int c = 64 * j + lane; glds(bx, (c / 20) * 1280 + (c % 20) * 16, mine + j * 1024); }
      const char* bw = (const char*)(isV ? P.wv : P.wk) + 6400 * w;
#pragma unroll
      for (int j = 0; j < 6; ++j) glds(bw + j * 1024, lane * 16, mine + 5120 + j * 1024);
      if (lane < 16) glds(bw + 6144, lane * 16, mine + 5120 + 6144); }
    bf16x8 gf[5];
#pragma unroll
    for (int s = 0; s < 5; ++s) asm volatile("global_load_dwordx4 %0, %1, %2" : "=v"(gf[s]) : "v"(g * 16), "s"((const char*)P.gw + 320 * w + 64 * s) : "memory");
    STAMP(11);
    wait_vm<0>();
    STAMP(12);
    asm volatile("" : "+v"(gf[0]), "+v"(gf[1]), "+v"(gf[2]), "+v"(gf[3]), "+v"(gf[4]));
    bf16x8 xb[5], a0[5], a1[5];
    const unsigned rW = mine + 5120 + (8 * g + q) * 40;
#pragma unroll
    for (int s = 0; s < 5; ++s) {
        xb[s] = lds128(mine + n * 320 + g * 16 + s * 64);
        a0[s] = cat(trd(rW + 8 * p + s * 1280), trd(rW + 8 * p + s * 1280 + 160)); a1[s] = cat(trd(rW + 32 + s * 1280), trd(rW + 32 + s * 1280 + 160));
    }
    SBAR();
    f32x4 e0 = {0, 0, 0, 0}, e1 = e0, e2 = e0;
    if (isV) {
#pragma unroll
        for (int s = 0; s < 5; ++s) { e0 = mfma16(xb[s], a0[s], e0); e1 = mfma16(xb[s], a1[s], e1); }
    } else {
#pragma unroll
        for (int s = 0; s < 5; ++s) { e0 = mfma16(a0[s], xb[s], e0); e1 = mfma16(a1[s], xb[s], e1); e2 = mfma16(gf[s], xb[s], e2); }
    }
    *(LAS f32x4*)(red + w * 1024 + lane * 16) = e0; *(LAS f32x4*)(red + 4096 + w * 1024 + lane * 16) = e1; *(LAS f32x4*)(red + 8192 + w * 1024 + lane * 16) = e2;
    STAMP(13);
    bar();
    STAMP(14);
    if (w < 3) {
        const unsigned r = red + w * 4096 + lane * 16;
        const f32x4 e = (*(LAS f32x4*)r + *(LAS f32x4*)(r + 1024)) + (*(LAS f32x4*)(r + 2048) + *(LAS f32x4*)(r + 3072));
        char* ck = P.ws + WS_KV + (tt >> 4) * KV_CHUNK;
        const int tl = (tt >> 1) & 7, sv = tt & 1;
        const u32x2 ev = {cvtpk(e[0], e[1]), cvtpk(e[2], e[3])};
        if (isV) {
            const unsigned vo = KV_V + tl * KV_VT + sv * 768 + (g & 1) * 8;
            if (w == 0) st8(ck, vo + (g >> 1) * 256 + n * 16, ev);
            else if (w == 1 && n < 8) st8(ck, vo + 512 + (g >> 1) * 128 + n * 16, n < 4 ? ev : (n == 4 ? (u32x2){0x3F803F80u, 0x3F803F80u} : (u32x2){0u, 0u}));
        } else {
            if (w == 0) st8(ck, tl * 1024 + (32 * (g >> 1) + 16 * sv + n) * 16 + (g & 1) * 8, ev);
            else if (w == 1) { if (g < 2) st4(ck, KV_K2 + tl * 256 + sv * 128 + g * 64 + n * 4, g ? ev[1] : ev[0]); }
            else if (g == 0) st4(P.ws + WS_G, (16 * tt + n) * 4, __float_as_uint(1.f / (1.f + __expf(-e[0]))));
        }
    }
}

DI void kv_stage(const char* kv, int c, unsigned lds, int w, int lane) {
#pragma unroll
    for (int j = 0; j < 5; ++j) glds_sc1(kv + c * KV_CHUNK + w * 5632 + j * 1024, lane * 16, lds + w * 5632 + j * 1024);
    if (lane < 32) glds_sc1(kv + c * KV_CHUNK + w * 5632 + 5120, lane * 16, lds + w * 5632 + 5120);
}
DI void attn_safe(const char* kv, int lane, const bf16x8 (&q1)[2], const unsigned (&q2)[2], f32x16 (&o)[2]) {
    const int d = lane & 31, ha = lane >> 5;
    const unsigned rK = lane * 16, rK2 = KV_K2 + (d >> 4) * 128 + ha * 64 + (d & 15) * 4, rV = KV_V + (d < 16 ? ha * 256 + d * 16 : 512 + ha * 128 + min(d - 16, 5) * 16);
#pragma unroll
    for (int t = 0; t < 2; ++t)
#pragma unroll
        for (int r = 0; r < 16; ++r) o[t][r] = 0.f;
    float m[2] = {-3.0e38f, -3.0e38f};
#pragma nounroll
    for (int c = 0; c < 8; ++c) {
#pragma nounroll
        for (int t = 0; t < 8; ++t) {
            bf16x8 k1, v0, v1; unsigned k2;
            asm volatile("global_load_dwordx4 %0, %4, %7 sc1\n\tglobal_load_dword %1, %5, %7 sc1\n\tglobal_load_dwordx4 %2, %6, %7 sc1\n\tglobal_load_dwordx4 %3, %6, %7 offset:768 sc1\n\ts_waitcnt vmcnt(0)"
                         : "=&v"(k1), "=&v"(k2), "=&v"(v0), "=&v"(v1) : "v"(rK + t * 1024), "v"(rK2 + t * 256), "v"(rV + t * KV_VT), "s"(kv + c * KV_CHUNK) : "memory");
#pragma unroll
            for (int u = 0; u < 2; ++u) {
                f32x16 s;
#pragma unroll
                for (int r = 0; r < 16; ++r) s[r] = 0.f;
                s = mfma32(k1, q1[u], s); s = mfma32k8((u32x2){k2, 0u}, (u32x2){q2[u], 0u}, s);
                float mx = s[0];
#pragma unroll
                for (int r = 1; r < 16; ++r) mx = fmaxf(mx, s[r]);
                auto rr = __builtin_amdgcn_permlane32_swap(__float_as_uint(mx), __float_as_uint(mx), false, false);
                mx = fmaxf(__uint_as_float(rr[0]), __uint_as_float(rr[1]));
                const float mn = fmaxf(m[u], mx), al = __builtin_amdgcn_exp2f(m[u] - mn); m[u] = mn;
#pragma unroll
                for (int r = 0; r < 16; ++r) { o[u][r] *= al; s[r] = __builtin_amdgcn_exp2f(s[r] - mn); }
                o[u] = smfmac(v0, __builtin_shufflevector(s, s, 0, 1, 2, 3, 4, 5, 6, 7), o[u], 0xDDDDDDDD);
                o[u] = smfmac(v1, __builtin_shufflevector(s, s, 8, 9, 10, 11, 12, 13, 14, 15), o[u], 0xDDDDDDDD);
            }
        }
    }
}
#define STR_(x) #x
#define STR(x) STR_(x)
#define R2(a) "v[" STR(a) ":" STR(a) "+1]"
#define R4(a) "v[" STR(a) ":" STR(a) "+3]"
#define R16(a) "v[" STR(a) ":" STR(a) "+15]"
#define E(r) "v_exp_f32 v[" STR(r) "], v[" STR(r) "]\n\t"
#define M32(N, K, Q) "v_mfma_f32_32x32x16_bf16 " R16(N) ", " R4(K) ", " Q ", 0\n\t"
#define M8(N, K, Q) "v_mfma_f32_32x32x8_bf16 " R16(N) ", " R2(K) ", " Q ", " R16(N) "\n\t"
#define SM(O, V, B) "v_smfmac_f32_32x32x32_bf16 " O ", " R4(V) ", v[" STR(B) ":" STR(B) "+7], %[idx]\n\t"
#define LK(d, off) "ds_read_b128 " R4(d) ", %[rK] offset:" STR(off) "\n\t"
#define LV(d, off) "ds_read_b128 " R4(d) ", %[rV] offset:" STR(off) "\n\t"
#define WL(n) "s_waitcnt lgkmcnt(" STR(n) ")\n\t"
#define SA0 192
#define SB0 208
#define SA1 224
#define SB1 240
#define K1A 160
#define K1B 164
#define K2A 168
#define K2B 170
#define V0A 176
#define V0B 180
#define V1A 184
#define V1B 188
#define STEP(N, C, Pv, K1, K2, Q1, Q2, VH, OH, VL, OL, XL, XM, X8, XH) \
    XL E(C) E(C+1) E(C+2) M32(N, K1, Q1) XM E(C+3) E(C+4) E(C+5) E(C+6) M8(N, K2, Q2) X8 E(C+7) E(C+8) E(C+9) E(C+10) SM(OH, VH, Pv+8) XH \
    E(C+11) E(C+12) E(C+13) E(C+14) E(C+15) SM(OL, VL, C)
#define LK2(d, off) "ds_read_b32 v[" STR(d) "], %[rK2] offset:" STR(off) "\n\t"
#define PAIR(VO1, VO2, T2, T3, A1M, A18, B1H, A2M, A28, A3M, A38, A4M, A48) \
    STEP(SB0, SA0, SB1, K1A, K2A, "%[q1b]", "%[q2b]", V1B, "%[o1]", V0A, "%[o0]", LV(V0B, VO1), A1M, A18, LV(V1B, VO1+768) B1H) \
    STEP(SA1, SB0, SA0, K1B, K2B, "%[q1a]", "%[q2a]", V1A, "%[o0]", V0A, "%[o1]", WL(1), LK2(K2A, T2*256) A2M, A28, LK(K1A, T2*1024)) \
    STEP(SB1, SA1, SB0, K1B, K2B, "%[q1b]", "%[q2b]", V1A, "%[o1]", V0B, "%[o0]", LV(V0A, VO2), A3M, A38, LV(V1A, VO2+768)) \
    STEP(SA0, SB1, SA1, K1A, K2A, "%[q1a]", "%[q2a]", V1B, "%[o0]", V0B, "%[o1]", WL(1), LK2(K2B, T3*256) A4M, A48, LK(K1B, T3*1024))
#define DRAIN SM("%[o1]", V1B, SB1+8)
#define ZINIT
#define DMA(off) "global_load_lds_dwordx4 %[voff], %[kvp] offset:" STR(off) " sc1\n\t"
#define DMA2(off) "global_load_lds_dwordx4 %[voff2], %[kvp] offset:" STR(off) " sc1\n\t"
#define CL10(a) "v" #a "0", "v" #a "1", "v" #a "2", "v" #a "3", "v" #a "4", "v" #a "5", "v" #a "6", "v" #a "7", "v" #a "8", "v" #a "9"
#define KCLOB CL10(16), CL10(17), CL10(18), CL10(19), CL10(20), CL10(21), CL10(22), CL10(23), CL10(24), "v250", "v251", "v252", "v253", "v254", "v255"
#define DSTAMP
#define DOPS
#define DCLOB
#define CHUNK_HEAD(CHK) \
    PAIR(1*1536, 2*1536, 2, 3, "", "", "", "", "", "", "", "", "") \
    PAIR(3*1536, 4*1536, 4, 5, "", "", "", "", "", "", "", "", "") \
    PAIR(5*1536, 6*1536, 6, 7, "", "", "", "", "", "", "", "", "") \
    CHK DSTAMP "s_cmp_eq_u32 %[slot], 2\n\ts_cselect_b32 %[dlt], %[dneg], %[dpos]\n\ts_waitcnt vmcnt(0)\n\t" DSTAMP "s_barrier\n\t" DSTAMP \
    "v_add_u32 %[rK], %[dlt], %[rK]\n\tv_add_u32 %[rK2], %[dlt], %[rK2]\n\ts_add_u32 %[slot], %[slot], 1\n\ts_cmp_eq_u32 %[slot], 3\n\ts_cselect_b32 %[slot], 0, %[slot]\n\t"
#define FCHK "v_cmp_ne_u64 vcc, %[tag], %[f8]\n\ts_cbranch_vccz LD%=\n\ts_mov_b32 %[dlt], 100000\n\t" \
    "LC%=:\n\tglobal_load_dwordx2 %[f8], %[vF], %[fl] sc1\n\ts_waitcnt vmcnt(0)\n\tv_cmp_ne_u64 vcc, %[tag], %[f8]\n\ts_cbranch_vccz LD%=\n\t" \
    "s_sleep 2\n\ts_sub_u32 %[dlt], %[dlt], 1\n\ts_cmp_lg_u32 %[dlt], 0\n\ts_cbranch_scc1 LC%=\n\tLD%=:\n\t"
#define VSW "v_add_u32 %[rV], %[dlt], %[rV]\n\t"
DI void attn_fast(const char* kv, const char* fl, int xc, u64 tag, u64 f8, unsigned lds0, int w, int lane, const bf16x8 (&q1)[2], const unsigned (&q2)[2], f32x16 (&o)[2]) {
    const int d = lane & 31, ha = lane >> 5;
    unsigned rK = lds0 + lane * 16, rK2 = lds0 + KV_K2 + (d >> 4) * 128 + ha * 64 + (d & 15) * 4, rV = lds0 + KV_V + (d < 16 ? ha * 256 + d * 16 : 512 + ha * 128 + min(d - 16, 5) * 16);
    const u32x2 q2a = {q2[0], 0u}, q2b = {q2[1], 0u};
    f32x16 o0, o1;
#pragma unroll
    for (int r = 0; r < 16; ++r) { o0[r] = 0.f; o1[r] = 0.f; }
    unsigned ci = (xc + 2) & 7;
    unsigned voff = ci * KV_CHUNK + w * 5632 + lane * 16, voff2 = voff + 4096;
    unsigned mdst = lds0 + 2 * KV_CHUNK + w * 5632, slot = 0, cnt = 6, dlt, mlo = lds0 + w * 5632;
    asm volatile(
        LK(K1A, 0) LK2(K2A, 0) LV(V0A, 0) LV(V1A, 768) LK(K1B, 1024) LK2(K2B, 256) "v_mov_b32 v169, 0\n\tv_mov_b32 v171, 0\n\t"
        "v_mov_b32 v248, 0\n\tv_mov_b32 v249, 0\n\tv_mov_b32 v250, 0\n\tv_mov_b32 v251, 0\n\tv_mov_b32 v252, 0\n\tv_mov_b32 v253, 0\n\tv_mov_b32 v254, 0\n\tv_mov_b32 v255, 0\n\t"
        "v_mov_b32 v188, 0\n\tv_mov_b32 v189, 0\n\tv_mov_b32 v190, 0\n\tv_mov_b32 v191, 0\n\t"
        ZINIT WL(0) M32(SA0, K1A, "%[q1a]") M8(SA0, K2A, "%[q2a]") "s_nop 15\n\t"
        "LA%=:\n\t"
        CHUNK_HEAD(FCHK)
        PAIR(7*1536, 0, 0, 1, "s_mov_b32 m0, %[mdst]\n\t", DMA(0), VSW,
             "", DMA(1024), DMA(2048), DMA(3072) "s_add_u32 m0, m0, 4096\n\t",
             DMA2(0), "s_mov_b32 exec_hi, 0\n\t" DMA2(1024) "s_mov_b32 exec_hi, -1\n\t")
        "s_cmp_eq_u32 %[ci], 7\n\ts_cselect_b32 %[dlt], %[dwrap], %[dpos]\n\ts_add_u32 %[ci], %[ci], 1\n\ts_and_b32 %[ci], %[ci], 7\n\t"
        "v_add_u32 %[voff], %[dlt], %[voff]\n\tv_add_u32 %[voff2], %[dlt], %[voff2]\n\t"
        "s_add_u32 %[mdst], %[mdst], 22528\n\ts_cmp_eq_u32 %[slot], 1\n\ts_cselect_b32 %[mdst], %[mlo], %[mdst]\n\t"
        "s_sub_u32 %[cnt], %[cnt], 1\n\ts_cmp_lg_u32 %[cnt], 0\n\ts_cbranch_scc1 LA%=\n\t"
        "s_mov_b32 %[cnt], 2\n\t"
        "LB%=:\n\t"
        CHUNK_HEAD("")
        PAIR(7*1536, 0, 0, 1, "", "", VSW, "", "", "", "", "", "")
        "s_sub_u32 %[cnt], %[cnt], 1\n\ts_cmp_lg_u32 %[cnt], 0\n\ts_cbranch_scc1 LB%=\n\t"
        "s_nop 1\n\t" DRAIN "s_waitcnt lgkmcnt(0)\n\ts_nop 15\n\ts_nop 3"
        : [o0] "+v"(o0), [o1] "+v"(o1), [rK] "+v"(rK), [rK2] "+v"(rK2), [rV] "+v"(rV), [voff] "+v"(voff), [voff2] "+v"(voff2), [mdst] "+s"(mdst), [slot] "+s"(slot), [cnt] "+s"(cnt), [dlt] "=&s"(dlt), [ci] "+s"(ci), [f8] "+v"(f8) DOPS
        : [q1a] "v"(q1[0]), [q1b] "v"(q1[1]), [q2a] "v"(q2a), [q2b] "v"(q2b), [idx] "v"(0xDDDDDDDD), [kvp] "s"(kv),
          [dneg] "s"(-2 * KV_CHUNK), [dpos] "s"(KV_CHUNK), [mlo] "s"(mlo), [dwrap] "s"(-7 * KV_CHUNK), [tag] "s"(tag), [fl] "s"(fl), [vF] "v"(FL_CH + (lane & 7) * FS)
        : "memory", "scc", "vcc", KCLOB DCLOB);
    o[0] = o0; o[1] = o1;
}

DI float upper_half(float v) { auto r = __builtin_amdgcn_permlane32_swap(__float_as_uint(v), __float_as_uint(v), false, false); return __uint_as_float(r[1]); }
DI void role_attn(const Params& P, int rb, int cb, unsigned lds0, int w, int lane, Tag& TG SARG) {
    u64 tag, f8;
    __builtin_amdgcn_s_setprio(2);
    const float gt = q_phase(P, rb, cb, lds0, w, lane, TG, tag, f8 SPASS);
    __builtin_amdgcn_s_setprio(A_PRIO);
    const char* kv = P.ws + WS_KV;
    const int n = lane & 31, h = lane >> 5, hp = w >> 1, T = 2 * rb + (w & 1);
    bf16x8 q1[2]; unsigned q2[2];
#pragma unroll
    for (int u = 0; u < 2; ++u) {
        const unsigned a = lds0 + L_Q + (32 * (w & 1) + n) * QROW + 80 * hp + 40 * u;
        q1[u] = cat(*(LAS s16x4*)(a + 16 * h), *(LAS s16x4*)(a + 16 * h + 8)); q2[u] = *(LAS unsigned*)(a + 32 + 4 * h);
    }
    f32x16 o[2];
    STAMP(5);
    attn_fast(kv, P.ws + WS_FL, rb >> 2, tag, f8, lds0, w, lane, q1, q2, o);
    __builtin_amdgcn_s_setprio(3);
    STAMP(6);
    char* O = P.ws + WS_O + (size_t)T * 40960 + (10 * cb + 5 * hp) * 512;
    auto store_o = [&](float l0, float l1) {
        const float i0 = gt * __builtin_amdgcn_rcpf(l0), i1 = gt * __builtin_amdgcn_rcpf(l1);
        unsigned A[3][2], B[3][2];
#pragma unroll
        for (int k = 0; k < 3; ++k) {
            A[k][0] = cvtpk(o[0][4 * k] * i0, o[0][4 * k + 1] * i0); A[k][1] = cvtpk(o[0][4 * k + 2] * i0, o[0][4 * k + 3] * i0);
            B[k][0] = cvtpk(o[1][4 * k] * i1, o[1][4 * k + 1] * i1); B[k][1] = cvtpk(o[1][4 * k + 2] * i1, o[1][4 * k + 3] * i1);
        }
        { auto r0 = __builtin_amdgcn_permlane32_swap(A[0][0], A[1][0], false, false); auto r1 = __builtin_amdgcn_permlane32_swap(A[0][1], A[1][1], false, false);
          st16(O, lane * 16, (u32x4){r0[0], r1[0], r0[1], r1[1]}); }
        if (!h) st16(O, 1024 + n * 16, (u32x4){A[2][0], A[2][1], B[0][0], B[0][1]});
        { auto r0 = __builtin_amdgcn_permlane32_swap(B[0][0], B[2][0], false, false); auto r1 = __builtin_amdgcn_permlane32_swap(B[0][1], B[2][1], false, false);
          st16(O, 1536 + lane * 16, h ? (u32x4){B[1][0], B[1][1], r0[0], r1[0]} : (u32x4){r0[1], r1[1], B[1][0], B[1][1]}); }
    };
    float l0 = upper_half(o[0][8]), l1 = upper_half(o[1][8]);
    float z = 0.f;
#pragma unroll
    for (int r = 0; r < 12; ++r) z = __builtin_fmaf(o[0][r], 0.f, __builtin_fmaf(o[1][r], 0.f, z));
    const bool bad = !(l0 > 0x1p-80f && l0 < 0x1p80f && l1 > 0x1p-80f && l1 < 0x1p80f && z == 0.f);
    if (__builtin_amdgcn_ballot_w64(bad) != 0) {
        attn_safe(kv, lane, q1, q2, o);
        l0 = upper_half(o[0][8]); l1 = upper_half(o[1][8]);
    }
    store_o(l0, l1);
    wait_vm<0>();
    if (lane == 0) __hip_atomic_store((GAS u64*)(P.ws + WS_FL + FL_O + (T * 16 + 2 * cb + hp) * FS), tag, __ATOMIC_RELAXED, __HIP_MEMORY_SCOPE_AGENT);
    STAMP(7);
}

struct OutCtx { unsigned voffW[4], ldsw; int rot; const char *wd, *wo; };
DI void out_stageW(const OutCtx& C, int st, int w) {
    const int v = st + C.rot;
    const char* b = C.wd + (unsigned)(v >= 12 ? v - 12 : v) * 40960u;
    set_m0(C.ldsw + (st % 5) * 4096); dma<0>(b, C.voffW[0]); dma<1024>(b, C.voffW[1]); dma<2048>(b, C.voffW[2]); dma<3072>(b, C.voffW[3]);
}
DI void out_init(OutCtx& C, const Params& P, int tb, int cj, unsigned lds0, int w, int lane) {
#pragma unroll
    for (int j = 0; j < 4; ++j) C.voffW[j] = (8 * j + (lane >> 3)) * 1280 + (((lane & 7) ^ (((lane >> 4) & 1) << 2)) * 16) + 3072 - 1024 * j;
    C.ldsw = lds0 + w * 20480; C.rot = (cj + 4 * (tb % 3)) % 12;
    C.wd = (const char*)P.wd + (size_t)(12 * w) * 40960 + cj * 128 - 3072; C.wo = (const char*)P.wo + (size_t)(5 * w) * 40960 + cj * 128 - 3072;
}
DI float gelu_mul(float a, float u) {
    const float t = a * a, z = a * (-2.302208198f - 0.1029432397f * t);
    return a * u * __builtin_amdgcn_rcpf(1.f + __builtin_amdgcn_exp2f(z));
}
template <class F> DI void ffnup_phase(const Params& P, int RB, int CB, int rot, unsigned lds0, int w, int lane, u64 tag, F&& mid SARG) {
    constexpr int L_W = 32768, WSL = 12288;
    const int wn = w & 1, wf = w >> 1;
    unsigned voffW[3], voffX[4];
#pragma unroll
    for (int j = 0; j < 3; ++j) { const int b = ((w & 1) * 3 + j) * 1024 + 16 * lane, r = b / 192, ch = ((b % 192) >> 4) ^ ((w >> 1) ? 2 : 0); voffW[j] = r * 3072 + ch * 16 + 2048 - 1024 * j; }
#pragma unroll
    for (int j = 0; j < 4; ++j) voffX[j] = (8 * j + (lane >> 3)) * 1280 + (((lane & 7) ^ ((lane >> 4) & 3) ^ ((j & 1) * 4)) * 16) + 3072 - 1024 * j;
    const char* baseW = (const char*)((w >> 1) ? P.wu : P.wg) + CB * 192 - 2048;
    const char* baseX = (const char*)P.x + (size_t)(128 * RB + 32 * w) * 1280 - 3072;
    const unsigned mW = __builtin_amdgcn_readfirstlane(lds0 + L_W + w * 3072), mX = __builtin_amdgcn_readfirstlane(lds0 + w * 4096);
    int kW = rot * 2, kX = rot;
    auto nextW = [&]() { const char* b = baseW + (unsigned)kW * (32u * 3072u); kW = kW == 19 ? 0 : kW + 1; return b; };
    auto nextX = [&]() { const char* b = baseX + (unsigned)kX * 128u; kX = kX == 9 ? 0 : kX + 1; return b; };
    { const char* b = nextX(); set_m0(mX); SBAR(); dma<0>(b, voffX[0]); dma<1024>(b, voffX[1]); dma<2048>(b, voffX[2]); dma<3072>(b, voffX[3]); }
#pragma unroll
    for (int i = 0; i < 3; ++i) { const char* b = nextW(); set_m0(mW + i * WSL); SBAR(); dma<0>(b, voffW[0]); dma<1024>(b, voffW[1]); dma<2048>(b, voffW[2]); }
    const char* bX = nextX();
    set_m0(mX + 16384); SBAR(); dma<0>(bX, voffX[0]); dma<1024>(bX, voffX[1]);
    const int h = lane >> 5, g = (lane >> 4) & 1, q = (lane >> 2) & 3, p = lane & 3, n = lane & 31;
    unsigned rB[4], rAw[3];
#pragma unroll
    for (int s = 0; s < 4; ++s) rB[s] = lds0 + (64 * wn + n) * 128 + (((2 * s + h) ^ ((n >> 1) & 7)) * 16);
#pragma unroll
    for (int a = 0; a < 3; ++a) { const int T = 3 * wf + a; rAw[a] = lds0 + L_W + g * 6144 + (8 * h + q) * 192 + (((T & 1) ^ g) * 32) + 8 * p + (T >> 1) * 64; }
    f32x16 acc[3][2];
#pragma unroll
    for (int a = 0; a < 3; ++a)
#pragma unroll
        for (int b = 0; b < 2; ++b)
#pragma unroll
            for (int r = 0; r < 16; ++r) acc[a][b][r] = 0.f;
    bf16x8 A0[3], B0[2], A1[3], B1[2];
    auto rdB = [&](bf16x8 (&B)[2], int i, int s) {
#pragma unroll
        for (int b = 0; b < 2; ++b) B[b] = lds128(rB[2 * (i & 1) + s] + ((i >> 1) & 1) * 16384 + b * 4096); };
    auto rdA = [&](bf16x8 (&A)[3], int a, int i, int s) { const unsigned ad = rAw[a] + (i & 3) * WSL + 16 * s * 192; A[a] = cat(trd(ad), trd(ad + 768)); };
    wait_bar<8>();
    rdB(B0, 0, 0); rdA(A0, 0, 0, 0); rdA(A0, 1, 0, 0); rdA(A0, 2, 0, 0);
    SBAR();
#define MM(A, B, a, b) acc[a][b] = mfma32(A[a], B[b], acc[a][b])
    auto stagef = [&](auto I, auto LW, auto LX, auto NV, auto NXT) {
        constexpr int i = I; constexpr bool lw = LW, lx = LX, nx = NXT; constexpr int nv = NV;
        const char* bW = nullptr;
        MM(A0, B0, 0, 0); rdB(B1, i, 1); SBAR();
        MM(A0, B0, 0, 1); rdA(A1, 0, i, 1); SBAR();
        MM(A0, B0, 1, 0); rdA(A1, 1, i, 1); if constexpr (lx && !(i & 1)) { set_m0(mX + (((i >> 1) + 1) & 1) * 16384); } SBAR();
        MM(A0, B0, 1, 1); rdA(A1, 2, i, 1); if constexpr (lx && !(i & 1)) { dmap<2048, UP_PRIO>(bX, voffX[2]); dmap<3072, UP_PRIO>(bX, voffX[3]); } if constexpr (lw) { bW = nextW(); set_m0(mW + ((i + 3) & 3) * WSL); } SBAR();
        MM(A0, B0, 2, 0); if constexpr (lw) dmap<0, UP_PRIO>(bW, voffW[0]); SBAR();
        MM(A0, B0, 2, 1); if constexpr (lw) dmap<1024, UP_PRIO>(bW, voffW[1]); SBAR();
        if constexpr (nx) wait_bar<nv>(); else wait_lgkm0();
        SBAR();
        MM(A1, B1, 0, 0); if constexpr (nx) rdB(B0, i + 1, 0); SBAR();
        MM(A1, B1, 0, 1); if constexpr (nx) rdA(A0, 0, i + 1, 0); SBAR();
        MM(A1, B1, 1, 0); if constexpr (nx) rdA(A0, 1, i + 1, 0); if constexpr (lw) dmap<2048, UP_PRIO>(bW, voffW[2]); SBAR();
        MM(A1, B1, 1, 1); if constexpr (nx) rdA(A0, 2, i + 1, 0); if constexpr (lx && (i & 1)) { bX = nextX(); set_m0(mX + (((i >> 1) + 2) & 1) * 16384); } SBAR();
        MM(A1, B1, 2, 0); if constexpr (lx && (i & 1)) dmap<0, UP_PRIO>(bX, voffX[0]); SBAR();
        MM(A1, B1, 2, 1); if constexpr (lx && (i & 1)) dmap<1024, UP_PRIO>(bX, voffX[1]); SBAR();
    };
    using T_ = std::true_type; using F_ = std::false_type;
#pragma nounroll
    for (int it = 0; it < 4; ++it) {
        stagef(IC(0), T_{}, T_{}, IC(9), T_{}); stagef(IC(1), T_{}, T_{}, IC(5), T_{}); stagef(IC(2), T_{}, T_{}, IC(9), T_{}); stagef(IC(3), T_{}, T_{}, IC(5), T_{});
    }
    stagef(IC(0), T_{}, T_{}, IC(9), T_{}); stagef(IC(1), F_{}, F_{}, IC(3), T_{}); stagef(IC(2), F_{}, F_{}, IC(0), T_{}); stagef(IC(3), F_{}, F_{}, IC(0), F_{});
#undef MM
    STAMP(2);
    bar(); HI(); mid();
#pragma unroll
    for (int a = 0; a < 3; ++a)
#pragma unroll
        for (int b = 0; b < 2; ++b) {
            const f32x16& c = acc[a][b];
            unsigned a0 = cvtpk(gelu_mul(c[0], c[8]), gelu_mul(c[1], c[9])), a1 = cvtpk(gelu_mul(c[2], c[10]), gelu_mul(c[3], c[11]));
            unsigned b0 = cvtpk(gelu_mul(c[4], c[12]), gelu_mul(c[5], c[13])), b1 = cvtpk(gelu_mul(c[6], c[14]), gelu_mul(c[7], c[15]));
            auto r0 = __builtin_amdgcn_permlane32_swap(a0, b0, false, false);
            auto r1 = __builtin_amdgcn_permlane32_swap(a1, b1, false, false);
            st16(P.ws + WS_H + (size_t)(4 * RB + 2 * wn + b) * 98304 + (6 * CB + 3 * wf + a) * 1024, lane * 16, (u32x4){r0[0], r1[0], r0[1], r1[1]});
        }
    publish(P.ws + WS_FL, FL_H + (RB * 16 + CB) * FS, tag);
}

DI void out_phase(const Params& P, const OutCtx& C, int tb, int cj, unsigned lds0, int w, int lane, u64 tag SARG) {
    const int n = lane & 31, h = lane >> 5, mh = (lane >> 4) & 1, q = (lane >> 2) & 3, p = lane & 3;
    const unsigned ldsw = C.ldsw;
    const char* fl = P.ws + WS_FL;
    unsigned vH[3];
#pragma unroll
    for (int t = 0; t < 3; ++t) { const int T = min(3 * tb + t, 63); vH[t] = T * 98304 + lane * 16; }
#define LDB(dst, base, vo, off) asm volatile("global_load_dwordx4 %0, %1, %2 offset:" #off " sc1" : "=v"(dst) : "v"(vo), "s"(base) : "memory")
#define LDBP(dst, base, vo, off) asm volatile("s_setprio " STR(D_BOOST) "\n\tglobal_load_dwordx4 %0, %1, %2 offset:" #off " sc1\n\ts_setprio " STR(G_PRIO) : "=v"(dst) : "v"(vo), "s"(base) : "memory")
#define PIN6(b) asm volatile("" : "+v"(b[0]), "+v"(b[1]), "+v"(b[2]), "+v"(b[3]), "+v"(b[4]), "+v"(b[5]))
    unsigned rA[2];
#pragma unroll
    for (int t = 0; t < 2; ++t) rA[t] = ldsw + (8 * h + q) * 128 + ((t ^ (q >> 1)) * 64) + mh * 32 + 8 * p;
    f32x16 acc[2][3];
#pragma unroll
    for (int a = 0; a < 2; ++a)
#pragma unroll
        for (int b = 0; b < 3; ++b)
#pragma unroll
            for (int r = 0; r < 16; ++r) acc[a][b][r] = 0.f;
    bf16x8 bf[3][6], af[2][4];
    auto rdA = [&](bf16x8 (&A)[4], int f, int slot) { const unsigned a = rA[f & 1] + slot * 4096 + (f >> 1) * 2048; A[f] = cat(trd(a), trd(a + 512)); };
    const char* Hb = P.ws + WS_H + 12 * w * 2048;
    auto hbase = [&](int st) { const int v = st + C.rot; return Hb + (unsigned)(v >= 12 ? v - 12 : v) * 2048u; };
    auto loadB = [&](const char* base, const unsigned (&vo)[3], bf16x8 (&b)[6]) { LDB(b[0], base, vo[0], 0); LDB(b[1], base, vo[1], 0); LDB(b[2], base, vo[2], 0); LDB(b[3], base, vo[0], 1024); LDB(b[4], base, vo[1], 1024); LDB(b[5], base, vo[2], 1024); };
    __builtin_amdgcn_s_setprio(0);
    { const int r0 = (96 * tb) >> 7, r1 = min((96 * tb + 95) >> 7, 15); wait_flags(fl, FL_H + (((lane & 4) ? r1 : r0) * 16 + 4 * w + (lane & 3)) * FS, tag); }
    HI();
    STAMP(4);
    loadB(hbase(0), vH, bf[0]); loadB(hbase(1), vH, bf[1]);
    __builtin_amdgcn_s_setprio(G_PRIO);
    rdA(af[0], 0, 0); rdA(af[0], 1, 0); rdA(af[0], 2, 0); rdA(af[0], 3, 0);
    wait_vm<6>();
#define MM(f, tt) acc[(f) & 1][tt] = mfma32(A[f], b[3 * ((f) >> 1) + tt], acc[(f) & 1][tt])
    auto step = [&](auto ST, auto LB, auto LW, auto RN, const char* nb, const unsigned (&vo)[3], auto BI) {
        constexpr int st = ST, bi = BI; constexpr bool lb = LB, lw = LW, rn = RN;
        bf16x8(&b)[6] = bf[st % 3]; bf16x8(&A)[4] = af[st & 1]; bf16x8(&An)[4] = af[(st + 1) & 1]; bf16x8(&bn)[6] = bf[bi];
        PIN6(b); SBAR();
        MM(0, 0); if constexpr (lb) LDBP(bn[0], nb, vo[0], 0); SBAR();
        MM(0, 1); if constexpr (rn) rdA(An, 0, (st + 1) % 5); SBAR();
        MM(0, 2); if constexpr (lb) LDBP(bn[1], nb, vo[1], 0); SBAR();
        MM(1, 0); if constexpr (rn) rdA(An, 1, (st + 1) % 5); SBAR();
        MM(1, 1); if constexpr (lb) LDBP(bn[2], nb, vo[2], 0); SBAR();
        MM(1, 2); if constexpr (rn) rdA(An, 2, (st + 1) % 5); SBAR();
        MM(2, 0); if constexpr (lb) LDBP(bn[3], nb, vo[0], 1024); SBAR();
        MM(2, 1); if constexpr (rn) rdA(An, 3, (st + 1) % 5); if constexpr (lb) LDBP(bn[4], nb, vo[1], 1024); SBAR();
        const char* wb = nullptr;
        MM(2, 2); if constexpr (lb) LDBP(bn[5], nb, vo[2], 1024);
        if constexpr (lw) { const int v = st + 5 + C.rot; wb = C.wd + (unsigned)(v >= 12 ? v - 12 : v) * 40960u; set_m0(ldsw + (st % 5) * 4096); dmap<0, G_PRIO, D_BOOST>(wb, C.voffW[0]); } SBAR();
        MM(3, 0); if constexpr (lw) dmap<1024, G_PRIO, D_BOOST>(wb, C.voffW[1]); SBAR();
        MM(3, 1); if constexpr (lw) dmap<2048, G_PRIO, D_BOOST>(wb, C.voffW[2]); SBAR();
        MM(3, 2); if constexpr (lw) dmap<3072, G_PRIO, D_BOOST>(wb, C.voffW[3]); SBAR();
    };
    sfor<0, 12>([&](auto ST) {
        constexpr int st = ST;
        step(ST, std::integral_constant<bool, (st + 2 < 12)>{}, std::integral_constant<bool, (st + 5 < 12)>{}, std::integral_constant<bool, (st + 1 < 12)>{}, hbase(st + 2 < 12 ? st + 2 : 0), vH, IC((st + 2) % 3));
        constexpr int later = (st >= 1 && st + 4 < 12 ? 4 : 0) + (st + 2 < 12 ? 6 : 0) + (st + 5 < 12 ? 4 : 0);
        if constexpr (st + 1 < 12) wait_vm<later>();
    });
#undef MM
    STAMP(5);
    wait_flags(fl, FL_CH + (lane & 7) * FS, tag);
    const int Tw = min(3 * tb + w, 63);
    const float gs = 1.f - ld4_sc1(P.ws + WS_G, (Tw * 32 + n) * 4);
    bar();
    f32x16 r0, r1;
    sfor<0, 3>([&](auto V) {
        constexpr int v = V;
        if (w != v) {
            const unsigned dst = lds0 + (v * 3 + (w > v ? w - 1 : w)) * 8192 + lane * 16;
#pragma unroll
            for (int a = 0; a < 2; ++a)
#pragma unroll
                for (int k = 0; k < 4; ++k) *(LAS f32x4*)(dst + (a * 4 + k) * 1024) = (f32x4){acc[a][v][4 * k], acc[a][v][4 * k + 1], acc[a][v][4 * k + 2], acc[a][v][4 * k + 3]};
        } else { r0 = acc[0][v]; r1 = acc[1][v]; }
    });
    bar();
    if (w < 3) {
#pragma unroll
        for (int sidx = 0; sidx < 3; ++sidx)
#pragma unroll
            for (int k = 0; k < 4; ++k) {
                const f32x4 a = *(LAS f32x4*)(lds0 + (w * 3 + sidx) * 8192 + k * 1024 + lane * 16), b = *(LAS f32x4*)(lds0 + (w * 3 + sidx) * 8192 + (4 + k) * 1024 + lane * 16);
#pragma unroll
                for (int e = 0; e < 4; ++e) { r0[4 * k + e] += a[e]; r1[4 * k + e] += b[e]; }
            }
        r0 *= gs; r1 *= gs;
    }
    bar();
    HI();
#pragma unroll
    for (int st = 0; st < 5; ++st) { const char* b = C.wo + st * 40960; set_m0(ldsw + st * 4096); dma<0>(b, C.voffW[0]); dma<1024>(b, C.voffW[1]); dma<2048>(b, C.voffW[2]); dma<3072>(b, C.voffW[3]); }
    wait_bar<0>();
    if (w == 3 || 3 * tb + w >= 64) return;
    __builtin_amdgcn_s_setprio(0);
    wait_flags<2>(fl, FL_O + (Tw * 16 + (lane & 15)) * FS, tag);
    __builtin_amdgcn_s_setprio(3);
    STAMP(6);
    const char* Ob = P.ws + WS_O + (size_t)Tw * 40960;
    bf16x8 ob[40];
    sfor<0, 10>([&](auto J) { constexpr int j = J; const char* b = Ob + j * 4096;
        ldb<0>(ob[4 * j], b, lane * 16); ldb<1024>(ob[4 * j + 1], b, lane * 16); ldb<2048>(ob[4 * j + 2], b, lane * 16); ldb<3072>(ob[4 * j + 3], b, lane * 16); });
    unsigned rW[2][2];
#pragma unroll
    for (int t = 0; t < 2; ++t) { rW[t][0] = rA[t] - ldsw + lds0; rW[t][1] = rW[t][0] + 40960; asm volatile("" : "+v"(rW[t][1])); }
    bf16x8 wf[2][4];
    auto rdW = [&](int kp) {
#pragma unroll
        for (int f = 0; f < 4; ++f) { const unsigned a = rW[f & 1][kp >= 10] + (kp % 10) * 4096 + (f >> 1) * 2048; wf[kp & 1][f] = cat(trd(a), trd(a + 512)); } };
    rdW(0);
    sfor<0, 20>([&](auto KP) {
        constexpr int kp = KP;
        if constexpr (kp + 1 < 20) rdW(kp + 1);
        wait_vm<38 - 2 * kp>();
        pin2(ob[2 * kp], ob[2 * kp + 1]);
        SBAR();
        r0 = mfma32(wf[kp & 1][0], ob[2 * kp], r0); r1 = mfma32(wf[kp & 1][1], ob[2 * kp], r1);
        r0 = mfma32(wf[kp & 1][2], ob[2 * kp + 1], r0); r1 = mfma32(wf[kp & 1][3], ob[2 * kp + 1], r1);
        SBAR();
    });
    STAMP(7);
    u16* orow = P.out + (size_t)(32 * Tw + n) * D_ + 64 * cj + 8 * h;
#pragma unroll
    for (int t = 0; t < 2; ++t) {
        const f32x16& r = t ? r1 : r0;
#pragma unroll
        for (int k = 0; k < 2; ++k) {
            auto a = __builtin_amdgcn_permlane32_swap(cvtpk(r[8 * k], r[8 * k + 1]), cvtpk(r[8 * k + 4], r[8 * k + 5]), false, false);
            auto b = __builtin_amdgcn_permlane32_swap(cvtpk(r[8 * k + 2], r[8 * k + 3]), cvtpk(r[8 * k + 6], r[8 * k + 7]), false, false);
            *(GAS u32x4*)(orow + 32 * t + 16 * k) = (u32x4){a[0], b[0], a[1], b[1]};
        }
    }
}

__global__ __launch_bounds__(256, 2) void fused_kernel(Params P) {
    extern __shared__ __attribute__((aligned(16))) char lds[];
    const unsigned lds0 = (unsigned)(uintptr_t)(LAS char*)lds;
    const int lane = threadIdx.x & 63, w = __builtin_amdgcn_readfirstlane(threadIdx.x >> 6);
    const int b = blockIdx.x & 255, xc = b & 7, idx = b >> 3;
    STAMP(0);
    asm volatile("" ::"s"(P.x), "s"(P.wq), "s"(P.wk), "s"(P.wv), "s"(P.wo), "s"(P.wg), "s"(P.wu), "s"(P.wd), "s"(P.gw), "s"(P.out), "s"(P.ws));
    Tag TG; TG.issue(P.ws + WS_FL);
    if (blockIdx.x < 256) role_attn(P, 4 * xc + (idx >> 3), idx & 7, lds0, w, lane, TG SPASS);
    else {
        __builtin_amdgcn_s_setprio(3);
        kv_job(P, 16 * xc + (idx >> 1), idx & 1, lds0, w, lane SPASS);
        const u64 tag = TG.get();
        STAMP(8);
        publish(P.ws + WS_FL, FL_KV + (32 * xc + idx) * FS, tag);
        if (idx == 0 && w == 0) {
            wait_flags<1>(P.ws + WS_FL, FL_KV + (32 * xc + (lane & 31)) * FS, tag);
            if (lane == 0) __hip_atomic_store((GAS u64*)(P.ws + WS_FL + FL_CH + xc * FS), tag, __ATOMIC_RELAXED, __HIP_MEMORY_SCOPE_AGENT);
        }
        __builtin_amdgcn_s_setprio(UP_PRIO);
        STAMP(9);
        if (G_DELAY) __builtin_amdgcn_s_sleep(G_DELAY);
        STAMP(1);
        const int l0 = (220 * xc) >> 3, l1 = (220 * (xc + 1)) >> 3, L = l0 + idx;
        OutCtx C;
        ffnup_phase(P, 8 * (xc >> 2) + (idx >> 2), 4 * (xc & 3) + (idx & 3), ((idx >> 2) + 3 * (idx & 3)) % 10, lds0, w, lane, tag,
                    [&]() { out_init(C, P, L / 10, L % 10, lds0, w, lane); if (L < l1) { out_stageW(C, 0, w); out_stageW(C, 1, w); out_stageW(C, 2, w); out_stageW(C, 3, w); out_stageW(C, 4, w); } } SPASS);
        STAMP(3);
        if (L < l1) out_phase(P, C, L / 10, L % 10, lds0, w, lane, tag SPASS);
        else if (b == 255 && w == 0) {
            __builtin_amdgcn_s_setprio(0);
#pragma nounroll
            for (int i = 0; i < 4; ++i) wait_flags<32>(P.ws + WS_FL, FL_H + (i * 64 + lane) * FS, tag);
#pragma nounroll
            for (int i = 0; i < 4; ++i) wait_flags<32>(P.ws + WS_FL, FL_O + ((i * 64 + lane) >> 3) * 32 * FS + ((i * 64 + lane) & 7) * 2 * FS, tag);
            if (lane == 0) __hip_atomic_store((GAS u64*)(P.ws + WS_FL), tag, __ATOMIC_RELAXED, __HIP_MEMORY_SCOPE_AGENT);
        }
    }
}

extern "C" void kernel_launch(void* const* d_in, const int* in_sizes, int n_in, void* d_out, int out_size, void* d_ws, size_t ws_size, hipStream_t stream) {
    Params P;
    P.x = (const u16*)d_in[0]; P.wq = (const u16*)d_in[1]; P.wk = (const u16*)d_in[2]; P.wv = (const u16*)d_in[3]; P.wo = (const u16*)d_in[4];
    P.wg = (const u16*)d_in[6]; P.wu = (const u16*)d_in[7]; P.wd = (const u16*)d_in[8]; P.gw = (const u16*)d_in[9];
    P.out = (u16*)d_out; P.ws = (char*)d_ws;
    (void)hipFuncSetAttribute((const void*)fused_kernel, hipFuncAttributeMaxDynamicSharedMemorySize, LDS_BYTES);
    fused_kernel<<<512, 256, LDS_BYTES, stream>>>(P);
}
```

```cpp
#define D_BOOST 0
#define UP_BOOST 0
#include <hip/hip_runtime.h>
#include <hip/hip_bf16.h>
#include <stdint.h>
#include <type_traits>

typedef unsigned short u16;
typedef unsigned long long u64;
typedef __attribute__((ext_vector_type(8))) __bf16 bf16x8;
typedef __attribute__((ext_vector_type(16))) __bf16 bf16x16;
typedef __attribute__((ext_vector_type(4))) short s16x4;
typedef __attribute__((ext_vector_type(4))) float f32x4;
typedef __attribute__((ext_vector_type(8))) float f32x8;
typedef __attribute__((ext_vector_type(16))) float f32x16;
typedef __attribute__((ext_vector_type(2))) unsigned u32x2;
typedef __attribute__((ext_vector_type(4))) unsigned u32x4;
#define LAS __attribute__((address_space(3)))
#define GAS __attribute__((address_space(1)))
#define DI __device__ __forceinline__

constexpr int D_ = 640;
constexpr int KV_CHUNK = 22528, KV_K2 = 8192, KV_V = 10240, KV_VT = 1536;
constexpr size_t WS_KV = 0, WS_G = 0x30000, WS_FL = 0x40000, WS_O = 1u << 20, WS_H = WS_O + 64 * 40960;
constexpr int FS = 256, FL_CH = 1024, FL_KV = 4096, FL_H = FL_KV + 256 * FS, FL_O = FL_H + 256 * FS;
constexpr float QSCALE = 0.22360679774997896f * 1.4426950408889634f;
constexpr int LDS_BYTES = 81920;
#define G_PRIO 0
#define UP_PRIO 0
#define A_PRIO 0
#define POLL_SLEEP 4
#define Q_PRIO 2
#define ST_PRIO 0
#define G_DELAY 0

#define STAMP(k)
#define SARG
#define SPASS

template <int N> DI void wait_vm() { asm volatile("s_waitcnt vmcnt(%0)" ::"n"(N) : "memory"); }
DI void wait_lgkm0() { asm volatile("s_waitcnt lgkmcnt(0)" ::: "memory"); }
template <int N> DI void wait_bar() { asm volatile("s_waitcnt vmcnt(%0) lgkmcnt(0)\n\ts_barrier" ::"n"(N) : "memory"); }
DI void bar() { asm volatile("s_waitcnt lgkmcnt(0)\n\ts_barrier" ::: "memory"); }
DI void glds(const void* sbase, unsigned voff, unsigned lds) {
    asm volatile("s_mov_b32 m0, %2\n\ts_nop 0\n\tglobal_load_lds_dwordx4 %0, %1" ::"v"(voff), "s"(sbase), "s"(lds) : "memory");
}
DI void glds_sc1(const void* sbase, unsigned voff, unsigned lds) {
    asm volatile("s_mov_b32 m0, %2\n\ts_nop 0\n\tglobal_load_lds_dwordx4 %0, %1 sc1" ::"v"(voff), "s"(sbase), "s"(lds) : "memory");
}
DI void set_m0(unsigned lds) { asm volatile("s_mov_b32 m0, %0\n\ts_nop 0" ::"s"(lds) : "memory"); }
template <int OFF, int LO, int HIP = 1> DI void dmap(const void* sbase, unsigned voff) { asm volatile("s_setprio %4\n\tglobal_load_lds_dwordx4 %0, %1 offset:%2\n\ts_setprio %3" ::"v"(voff), "s"(sbase), "n"(OFF), "n"(LO), "n"(HIP) : "memory"); }
template <int OFF> DI void dma(const void* sbase, unsigned voff) { asm volatile("global_load_lds_dwordx4 %0, %1 offset:%2" ::"v"(voff), "s"(sbase), "n"(OFF) : "memory"); }
#define SBAR() __builtin_amdgcn_sched_barrier(0)
#define HI() __builtin_amdgcn_s_setprio(2)
DI void st16(char* sbase, unsigned voff, u32x4 v) { asm volatile("global_store_dwordx4 %0, %1, %2 sc1\n\ts_nop 1" ::"v"(voff), "v"(v), "s"(sbase) : "memory"); }
DI void st8(char* sbase, unsigned voff, u32x2 v) { asm volatile("global_store_dwordx2 %0, %1, %2 sc1" ::"v"(voff), "v"(v), "s"(sbase) : "memory"); }
DI void st4(char* sbase, unsigned voff, unsigned v) { asm volatile("global_store_dword %0, %1, %2 sc1" ::"v"(voff), "v"(v), "s"(sbase) : "memory"); }
DI float ld4_sc1(const char* sbase, unsigned voff) { float r; asm volatile("global_load_dword %0, %1, %2 sc1\n\ts_waitcnt vmcnt(0)" : "=&v"(r) : "v"(voff), "s"(sbase) : "memory"); return r; }
template <int OFF> DI void ldb(bf16x8& d, const char* sbase, unsigned voff) { asm volatile("global_load_dwordx4 %0, %1, %2 offset:%3 sc1" : "=v"(d) : "v"(voff), "s"(sbase), "n"(OFF) : "memory"); }
DI void pin2(bf16x8& a, bf16x8& b) { asm volatile("" : "+v"(a), "+v"(b)); }
typedef __attribute__((ext_vector_type(2))) float f32x2;
typedef __attribute__((ext_vector_type(2))) __bf16 bf16x2;
DI unsigned cvtpk(float lo, float hi) { return __builtin_bit_cast(unsigned, __builtin_convertvector((f32x2){lo, hi}, bf16x2)); }
DI s16x4 trd(unsigned a) { return __builtin_amdgcn_ds_read_tr16_b64_v4i16((LAS s16x4*)a); }
DI bf16x8 cat(s16x4 a, s16x4 b) { return __builtin_bit_cast(bf16x8, __builtin_shufflevector(a, b, 0, 1, 2, 3, 4, 5, 6, 7)); }
DI bf16x8 lds128(unsigned a) { return *(LAS bf16x8*)a; }
DI f32x4 mfma16(bf16x8 a, bf16x8 b, f32x4 c) { return __builtin_amdgcn_mfma_f32_16x16x32_bf16(a, b, c, 0, 0, 0); }
DI f32x16 mfma32(bf16x8 a, bf16x8 b, f32x16 c) { return __builtin_amdgcn_mfma_f32_32x32x16_bf16(a, b, c, 0, 0, 0); }
DI f32x16 mfma32k8(u32x2 a, u32x2 b, f32x16 c) { return __builtin_amdgcn_mfma_f32_32x32x8bf16_1k(__builtin_bit_cast(s16x4, a), __builtin_bit_cast(s16x4, b), c, 0, 0, 0); }
DI f32x16 smfmac(bf16x8 a, f32x8 b, f32x16 c, int idx) { return __builtin_amdgcn_smfmac_f32_32x32x32_bf16(a, __builtin_bit_cast(bf16x16, b), c, idx, 0, 0); }
template <int I, int N, class F> DI void sfor(F&& f) { if constexpr (I < N) { f(std::integral_constant<int, I>{}); sfor<I + 1, N>(f); } }
DI int krow_of(int rho) { const int g = ((rho >> 4) << 1) | ((rho >> 2) & 1); return 8 * g + 4 * ((rho >> 3) & 1) + (rho & 3); }
DI int mod10(int v) { return v >= 10 ? v - 10 : v; }

struct Tag { u64 e; DI void issue(const char* fl) { asm volatile("s_load_dwordx2 %0, %1, 0x0" : "=s"(e) : "s"(fl) : "memory"); }
    DI u64 get() { asm volatile("s_waitcnt lgkmcnt(0)" : "+s"(e) :: "memory"); return e * 6364136223846793005ull + 1442695040888963407ull; } };
struct Params {
    const u16 *x, *wq, *wk, *wv, *wo, *wg, *wu, *wd, *gw;
    u16* out; char* ws;
};

DI void publish(char* fl, unsigned off, u64 tag) {
    asm volatile("s_waitcnt vmcnt(0)\n\ts_barrier" ::: "memory");
    if (threadIdx.x == 0) __hip_atomic_store((GAS u64*)(fl + off), tag, __ATOMIC_RELAXED, __HIP_MEMORY_SCOPE_AGENT);
}
template <int SLEEP = POLL_SLEEP> DI void wait_flags(const char* fl, unsigned voff, u64 tag) {
#pragma nounroll
    for (int spin = 0; spin < 100000; ++spin) {
        u64 v; asm volatile("global_load_dwordx2 %0, %1, %2 sc1\n\ts_waitcnt vmcnt(0)" : "=&v"(v) : "v"(voff), "s"(fl) : "memory");
        if (__builtin_amdgcn_ballot_w64(v != tag) == 0) break;
        __builtin_amdgcn_s_sleep(SLEEP);
    }
}

DI void kv_stage(const char* kv, int c, unsigned lds, int w, int lane);
constexpr unsigned L_Q = 2 * KV_CHUNK, QROW = 168;
#define IC(v) std::integral_constant<int, v>{}
DI float q_phase(const Params& P, int rb, int cb, unsigned lds0, int w, int lane, Tag& TG, u64& tag, u64& f0 SARG) {
    const int xc = rb >> 2;
    const int n = lane & 15, g = lane >> 4, q = (lane >> 2) & 3, p = lane & 3;
    constexpr unsigned SL = 18432;
    const unsigned vX0 = (lane >> 3) * 1280 + (((lane & 7) ^ ((lane >> 4) & 3)) * 16) + 1024, vX1 = (vX0 ^ 64) + 8 * 1280 - 1024;
    unsigned vW[3];
#pragma unroll
    for (int j = 0; j < 3; ++j) { const int b = j * 1024 + 16 * lane, r = b / 160, ch = (b % 160) >> 4; vW[j] = krow_of((16 * (w & 1) + r) & 31) * 1280 + ch * 16 + 2048 - 1024 * j; }
    const char* baseX = (const char*)P.x + (size_t)(64 * rb + 16 * w) * 1280 - 1024;
    const char* baseW = (const char*)P.wq + (size_t)(32 * (w >> 1)) * 1280 + cb * 160 - 2048;
    const unsigned mX = lds0 + w * 2048, mW = lds0 + 8192 + w * 2560;
    int ku = (cb + 3 * (rb & 3)) % 10;
    const char *bX, *bW;
    auto nextU = [&]() { bX = baseX + ku * 128; bW = baseW + (unsigned)ku * 81920u; ku = ku == 9 ? 0 : ku + 1; };
    auto dX = [&](int u) { set_m0(mX + ((u + 2) & 3) * SL); dma<0>(bX, vX0); dma<1024>(bX, vX1); };
    auto dW0 = [&](int u) { set_m0(mW + ((u + 2) & 3) * SL); dma<0>(bW, vW[0]); };
    auto dW2 = [&]() { asm volatile("s_mov_b32 exec_hi, 0\n\tglobal_load_lds_dwordx4 %0, %1 offset:2048\n\ts_mov_b32 exec_hi, -1" ::"v"(vW[2]), "s"(bW) : "memory"); };
#pragma unroll
    for (int u = 0; u < 3; ++u) { nextU(); dX(u); dW0(u); dma<1024>(bW, vW[1]); dW2(); }
    const unsigned rA = lds0 + 8192 + ((g >> 1) * 16 + (g & 1) * 4 + q) * 160 + 8 * p, rB = lds0 + (16 * w + n) * 128 + ((g ^ ((n >> 1) & 7)) * 16);
    unsigned rA3 = rA + 3 * SL, rB1 = rB ^ 64; asm volatile("" : "+v"(rA3), "+v"(rB1));
    f32x4 acc[5];
#pragma unroll
    for (int t = 0; t < 5; ++t) acc[t] = (f32x4){0, 0, 0, 0};
    bf16x8 F[2][5], X[2];
    auto rdX = [&](int u, int s) { X[s] = lds128((s ? rB1 : rB) + ((u + 2) & 3) * SL); };
    auto rdF = [&](int u, int s, int t) { const unsigned a = (((u + 2) & 3) == 3 ? rA3 : rA + ((u + 2) & 3) * SL) + s * 5120 + t * 32; F[s][t] = cat(trd(a), trd(a + 1280)); };
    const char* fl = P.ws + WS_FL; const char* kvp = P.ws + WS_KV;
    bool early = false;
    const unsigned vF = FL_CH + (((lane & 7) + xc) & 7) * FS;
    STAMP(8);
    wait_bar<10>();
    tag = TG.get();
    STAMP(9);
    rdX(0, 0); rdF(0, 0, 0); rdF(0, 0, 1); rdF(0, 0, 2); rdF(0, 0, 3); rdF(0, 0, 4);
    SBAR();
#define MM(s, t) acc[t] = mfma16(F[s][t], X[s], acc[t])
    sfor<0, 10>([&](auto U) {
        constexpr int u = U; constexpr bool ld = u + 3 < 10, nx = u + 1 < 10;
        if constexpr (ld) nextU();
        MM(0, 0); rdX(u, 1); rdF(u, 1, 0); SBAR();
        MM(0, 1); rdF(u, 1, 1); rdF(u, 1, 2); SBAR();
        MM(0, 2); rdF(u, 1, 3); rdF(u, 1, 4); SBAR();
        MM(0, 3); if constexpr (ld) dX(u + 3); SBAR();
        MM(0, 4); SBAR();
        if constexpr (u == 8) { wait_vm<0>(); asm volatile("" : "+v"(f0)); early = (__builtin_amdgcn_ballot_w64(f0 != tag) & 3) == 0; }
        if constexpr (nx) { if constexpr (u <= 6) wait_bar<7>(); else if constexpr (u == 7) wait_bar<5>(); else wait_bar<0>(); } else wait_lgkm0();
        SBAR();
        MM(1, 0); if constexpr (nx) { rdX(u + 1, 0); rdF(u + 1, 0, 0); } SBAR();
        MM(1, 1); if constexpr (nx) { rdF(u + 1, 0, 1); rdF(u + 1, 0, 2); } SBAR();
        MM(1, 2); if constexpr (nx) { rdF(u + 1, 0, 3); rdF(u + 1, 0, 4); } SBAR();
        MM(1, 3); if constexpr (ld) { dW0(u + 3); dma<1024>(bW, vW[1]); } SBAR();
        MM(1, 4); if constexpr (ld) dW2(); SBAR();
        if constexpr (u == 7) asm volatile("global_load_dwordx2 %0, %1, %2 sc1" : "=v"(f0) : "v"(vF), "s"(fl) : "memory");
        if constexpr (u == 8) { if (early) kv_stage(kvp, xc, lds0, w, lane); }
    });
#undef MM
    STAMP(3);
    if (!early) { wait_flags<2>(fl, FL_CH + (((lane & 1) + xc) & 7) * FS, tag); kv_stage(kvp, xc, lds0, w, lane); }
    bar();
    float gt; asm volatile("global_load_dword %0, %1, %2 sc1" : "=v"(gt) : "v"((64 * rb + 32 * (w & 1) + (lane & 31)) * 4), "s"(P.ws + WS_G) : "memory");
    kv_stage(kvp, (xc + 1) & 7, lds0 + KV_CHUNK, w, lane);
    { const unsigned a = lds0 + L_Q + (16 * w + n) * QROW + g * 8;
#pragma unroll
      for (int t = 0; t < 5; ++t) *(LAS u32x2*)(a + 32 * t) = (u32x2){cvtpk(acc[t][0] * QSCALE, acc[t][1] * QSCALE), cvtpk(acc[t][2] * QSCALE, acc[t][3] * QSCALE)}; }
    STAMP(4);
    wait_bar<6>();
    asm volatile("" : "+v"(gt));
    return gt;
}

DI void kv_job(const Params& P, int tt, bool isV, unsigned lds0, int w, int lane SARG) {
    const int n = lane & 15, g = lane >> 4, q = (lane >> 2) & 3, p = lane & 3;
    const unsigned mine = lds0 + w * 12288, red = lds0 + 69632;
    { const char* bx = (const char*)P.x + (size_t)(16 * tt) * 1280 + 320 * w;
  #pragma unroll
      for (int j = 0; j < 5; ++j) { const int c = 64 * j + lane; glds(bx, (c / 20) * 1280 + (c % 20) * 16, mine + j * 1024); }
      const char* bw = (const char*)(isV ? P.wv : P.wk) + 6400 * w;
#pragma unroll
      for (int j = 0; j < 6; ++j) glds(bw + j * 1024, lane * 16, mine + 5120 + j * 1024);
      if (lane < 16) glds(bw + 6144, lane * 16, mine + 5120 + 6144); }
    bf16x8 gf[5];
#pragma unroll
    for (int s = 0; s < 5; ++s) asm volatile("global_load_dwordx4 %0, %1, %2" : "=v"(gf[s]) : "v"(g * 16), "s"((const char*)P.gw + 320 * w + 64 * s) : "memory");
    wait_vm<0>();
    asm volatile("" : "+v"(gf[0]), "+v"(gf[1]), "+v"(gf[2]), "+v"(gf[3]), "+v"(gf[4]));
    bf16x8 xb[5], a0[5], a1[5];
    const unsigned rW = mine + 5120 + (8 * g + q) * 40;
#pragma unroll
    for (int s = 0; s < 5; ++s) {
        xb[s] = lds128(mine + n * 320 + g * 16 + s * 64);
        a0[s] = cat(trd(rW + 8 * p + s * 1280), trd(rW + 8 * p + s * 1280 + 160)); a1[s] = cat(trd(rW + 32 + s * 1280), trd(rW + 32 + s * 1280 + 160));
    }
    SBAR();
    f32x4 e0 = {0, 0, 0, 0}, e1 = e0, e2 = e0;
    if (isV) {
#pragma unroll
        for (int s = 0; s < 5; ++s) { e0 = mfma16(xb[s], a0[s], e0); e1 = mfma16(xb[s], a1[s], e1); }
    } else {
#pragma unroll
        for (int s = 0; s < 5; ++s) { e0 = mfma16(a0[s], xb[s], e0); e1 = mfma16(a1[s], xb[s], e1); e2 = mfma16(gf[s], xb[s], e2); }
    }
    *(LAS f32x4*)(red + w * 1024 + lane * 16) = e0; *(LAS f32x4*)(red + 4096 + w * 1024 + lane * 16) = e1; *(LAS f32x4*)(red + 8192 + w * 1024 + lane * 16) = e2;
    bar();
    if (w < 3) {
        const unsigned r = red + w * 4096 + lane * 16;
        const f32x4 e = (*(LAS f32x4*)r + *(LAS f32x4*)(r + 1024)) + (*(LAS f32x4*)(r + 2048) + *(LAS f32x4*)(r + 3072));
        char* ck = P.ws + WS_KV + (tt >> 4) * KV_CHUNK;
        const int tl = (tt >> 1) & 7, sv = tt & 1;
        const u32x2 ev = {cvtpk(e[0], e[1]), cvtpk(e[2], e[3])};
        if (isV) {
            const unsigned vo = KV_V + tl * KV_VT + sv * 768 + (g & 1) * 8;
            if (w == 0) st8(ck, vo + (g >> 1) * 256 + n * 16, ev);
            else if (w == 1 && n < 8) st8(ck, vo + 512 + (g >> 1) * 128 + n * 16, n < 4 ? ev : (n == 4 ? (u32x2){0x3F803F80u, 0x3F803F80u} : (u32x2){0u, 0u}));
        } else {
            if (w == 0) st8(ck, tl * 1024 + (32 * (g >> 1) + 16 * sv + n) * 16 + (g & 1) * 8, ev);
            else if (w == 1) { if (g < 2) st4(ck, KV_K2 + tl * 256 + sv * 128 + g * 64 + n * 4, g ? ev[1] : ev[0]); }
            else if (g == 0) st4(P.ws + WS_G, (16 * tt + n) * 4, __float_as_uint(1.f / (1.f + __expf(-e[0]))));
        }
    }
}

DI void kv_stage(const char* kv, int c, unsigned lds, int w, int lane) {
#pragma unroll
    for (int j = 0; j < 5; ++j) glds_sc1(kv + c * KV_CHUNK + w * 5632 + j * 1024, lane * 16, lds + w * 5632 + j * 1024);
    if (lane < 32) glds_sc1(kv + c * KV_CHUNK + w * 5632 + 5120, lane * 16, lds + w * 5632 + 5120);
}
DI void attn_safe(const char* kv, int lane, const bf16x8 (&q1)[2], const unsigned (&q2)[2], f32x16 (&o)[2]) {
    const int d = lane & 31, ha = lane >> 5;
    const unsigned rK = lane * 16, rK2 = KV_K2 + (d >> 4) * 128 + ha * 64 + (d & 15) * 4, rV = KV_V + (d < 16 ? ha * 256 + d * 16 : 512 + ha * 128 + min(d - 16, 5) * 16);
#pragma unroll
    for (int t = 0; t < 2; ++t)
#pragma unroll
        for (int r = 0; r < 16; ++r) o[t][r] = 0.f;
    float m[2] = {-3.0e38f, -3.0e38f};
#pragma nounroll
    for (int c = 0; c < 8; ++c) {
#pragma nounroll
        for (int t = 0; t < 8; ++t) {
            bf16x8 k1, v0, v1; unsigned k2;
            asm volatile("global_load_dwordx4 %0, %4, %7 sc1\n\tglobal_load_dword %1, %5, %7 sc1\n\tglobal_load_dwordx4 %2, %6, %7 sc1\n\tglobal_load_dwordx4 %3, %6, %7 offset:768 sc1\n\ts_waitcnt vmcnt(0)"
                         : "=&v"(k1), "=&v"(k2), "=&v"(v0), "=&v"(v1) : "v"(rK + t * 1024), "v"(rK2 + t * 256), "v"(rV + t * KV_VT), "s"(kv + c * KV_CHUNK) : "memory");
#pragma unroll
            for (int u = 0; u < 2; ++u) {
                f32x16 s;
#pragma unroll
                for (int r = 0; r < 16; ++r) s[r] = 0.f;
                s = mfma32(k1, q1[u], s); s = mfma32k8((u32x2){k2, 0u}, (u32x2){q2[u], 0u}, s);
                float mx = s[0];
#pragma unroll
                for (int r = 1; r < 16; ++r) mx = fmaxf(mx, s[r]);
                auto rr = __builtin_amdgcn_permlane32_swap(__float_as_uint(mx), __float_as_uint(mx), false, false);
                mx = fmaxf(__uint_as_float(rr[0]), __uint_as_float(rr[1]));
                const float mn = fmaxf(m[u], mx), al = __builtin_amdgcn_exp2f(m[u] - mn); m[u] = mn;
#pragma unroll
                for (int r = 0; r < 16; ++r) { o[u][r] *= al; s[r] = __builtin_amdgcn_exp2f(s[r] - mn); }
                o[u] = smfmac(v0, __builtin_shufflevector(s, s, 0, 1, 2, 3, 4, 5, 6, 7), o[u], 0xDDDDDDDD);
                o[u] = smfmac(v1, __builtin_shufflevector(s, s, 8, 9, 10, 11, 12, 13, 14, 15), o[u], 0xDDDDDDDD);
            }
        }
    }
}
#define STR_(x) #x
#define STR(x) STR_(x)
#define R2(a) "v[" STR(a) ":" STR(a) "+1]"
#define R4(a) "v[" STR(a) ":" STR(a) "+3]"
#define R16(a) "v[" STR(a) ":" STR(a) "+15]"
#define E(r) "v_exp_f32 v[" STR(r) "], v[" STR(r) "]\n\t"
#define M32(N, K, Q) "v_mfma_f32_32x32x16_bf16 " R16(N) ", " R4(K) ", " Q ", 0\n\t"
#define M8(N, K, Q) "v_mfma_f32_32x32x8_bf16 " R16(N) ", " R2(K) ", " Q ", " R16(N) "\n\t"
#define SM(O, V, B) "v_smfmac_f32_32x32x32_bf16 " O ", " R4(V) ", v[" STR(B) ":" STR(B) "+7], %[idx]\n\t"
#define LK(d, off) "ds_read_b128 " R4(d) ", %[rK] offset:" STR(off) "\n\t"
#define LV(d, off) "ds_read_b128 " R4(d) ", %[rV] offset:" STR(off) "\n\t"
#define WL(n) "s_waitcnt lgkmcnt(" STR(n) ")\n\t"
#define SA0 192
#define SB0 208
#define SA1 224
#define SB1 240
#define K1A 160
#define K1B 164
#define K2A 168
#define K2B 170
#define V0A 176
#define V0B 180
#define V1A 184
#define V1B 188
#define STEP(N, C, Pv, K1, K2, Q1, Q2, VH, OH, VL, OL, XL, XM, X8, XH) \
    XL E(C) E(C+1) E(C+2) M32(N, K1, Q1) XM E(C+3) E(C+4) E(C+5) E(C+6) M8(N, K2, Q2) X8 E(C+7) E(C+8) E(C+9) E(C+10) SM(OH, VH, Pv+8) XH \
    E(C+11) E(C+12) E(C+13) E(C+14) E(C+15) SM(OL, VL, C)
#define LK2(d, off) "ds_read_b32 v[" STR(d) "], %[rK2] offset:" STR(off) "\n\t"
#define PAIR(VO1, VO2, T2, T3, A1M, A18, B1H, A2M, A28, A3M, A38, A4M, A48) \
    STEP(SB0, SA0, SB1, K1A, K2A, "%[q1b]", "%[q2b]", V1B, "%[o1]", V0A, "%[o0]", LV(V0B, VO1), A1M, A18, LV(V1B, VO1+768) B1H) \
    STEP(SA1, SB0, SA0, K1B, K2B, "%[q1a]", "%[q2a]", V1A, "%[o0]", V0A, "%[o1]", WL(1), LK2(K2A, T2*256) A2M, A28, LK(K1A, T2*1024)) \
    STEP(SB1, SA1, SB0, K1B, K2B, "%[q1b]", "%[q2b]", V1A, "%[o1]", V0B, "%[o0]", LV(V0A, VO2), A3M, A38, LV(V1A, VO2+768)) \
    STEP(SA0, SB1, SA1, K1A, K2A, "%[q1a]", "%[q2a]", V1B, "%[o0]", V0B, "%[o1]", WL(1), LK2(K2B, T3*256) A4M, A48, LK(K1B, T3*1024))
#define DRAIN SM("%[o1]", V1B, SB1+8)
#define ZINIT
#define DMA(off) "global_load_lds_dwordx4 %[voff], %[kvp] offset:" STR(off) " sc1\n\t"
#define DMA2(off) "global_load_lds_dwordx4 %[voff2], %[kvp] offset:" STR(off) " sc1\n\t"
#define CL10(a) "v" #a "0", "v" #a "1", "v" #a "2", "v" #a "3", "v" #a "4", "v" #a "5", "v" #a "6", "v" #a "7", "v" #a "8", "v" #a "9"
#define KCLOB CL10(16), CL10(17), CL10(18), CL10(19), CL10(20), CL10(21), CL10(22), CL10(23), CL10(24), "v250", "v251", "v252", "v253", "v254", "v255"
#define DSTAMP
#define DOPS
#define DCLOB
#define CHUNK_HEAD(CHK) \
    PAIR(1*1536, 2*1536, 2, 3, "", "", "", "", "", "", "", "", "") \
    PAIR(3*1536, 4*1536, 4, 5, "", "", "", "", "", "", "", "", "") \
    PAIR(5*1536, 6*1536, 6, 7, "", "", "", "", "", "", "", "", "") \
    CHK DSTAMP "s_cmp_eq_u32 %[slot], 2\n\ts_cselect_b32 %[dlt], %[dneg], %[dpos]\n\ts_waitcnt vmcnt(0)\n\t" DSTAMP "s_barrier\n\t" DSTAMP \
    "v_add_u32 %[rK], %[dlt], %[rK]\n\tv_add_u32 %[rK2], %[dlt], %[rK2]\n\ts_add_u32 %[slot], %[slot], 1\n\ts_cmp_eq_u32 %[slot], 3\n\ts_cselect_b32 %[slot], 0, %[slot]\n\t"
#define FCHK "v_cmp_ne_u64 vcc, %[tag], %[f8]\n\ts_cbranch_vccz LD%=\n\ts_mov_b32 %[dlt], 100000\n\t" \
    "LC%=:\n\tglobal_load_dwordx2 %[f8], %[vF], %[fl] sc1\n\ts_waitcnt vmcnt(0)\n\tv_cmp_ne_u64 vcc, %[tag], %[f8]\n\ts_cbranch_vccz LD%=\n\t" \
    "s_sleep 2\n\ts_sub_u32 %[dlt], %[dlt], 1\n\ts_cmp_lg_u32 %[dlt], 0\n\ts_cbranch_scc1 LC%=\n\tLD%=:\n\t"
#define VSW "v_add_u32 %[rV], %[dlt], %[rV]\n\t"
DI void attn_fast(const char* kv, const char* fl, int xc, u64 tag, u64 f8, unsigned lds0, int w, int lane, const bf16x8 (&q1)[2], const unsigned (&q2)[2], f32x16 (&o)[2]) {
    const int d = lane & 31, ha = lane >> 5;
    unsigned rK = lds0 + lane * 16, rK2 = lds0 + KV_K2 + (d >> 4) * 128 + ha * 64 + (d & 15) * 4, rV = lds0 + KV_V + (d < 16 ? ha * 256 + d * 16 : 512 + ha * 128 + min(d - 16, 5) * 16);
    const u32x2 q2a = {q2[0], 0u}, q2b = {q2[1], 0u};
    f32x16 o0, o1;
#pragma unroll
    for (int r = 0; r < 16; ++r) { o0[r] = 0.f; o1[r] = 0.f; }
    unsigned ci = (xc + 2) & 7;
    unsigned voff = ci * KV_CHUNK + w * 5632 + lane * 16, voff2 = voff + 4096;
    unsigned mdst = lds0 + 2 * KV_CHUNK + w * 5632, slot = 0, cnt = 6, dlt, mlo = lds0 + w * 5632;
    asm volatile(
        LK(K1A, 0) LK2(K2A, 0) LV(V0A, 0) LV(V1A, 768) LK(K1B, 1024) LK2(K2B, 256) "v_mov_b32 v169, 0\n\tv_mov_b32 v171, 0\n\t"
        "v_mov_b32 v248, 0\n\tv_mov_b32 v249, 0\n\tv_mov_b32 v250, 0\n\tv_mov_b32 v251, 0\n\tv_mov_b32 v252, 0\n\tv_mov_b32 v253, 0\n\tv_mov_b32 v254, 0\n\tv_mov_b32 v255, 0\n\t"
        "v_mov_b32 v188, 0\n\tv_mov_b32 v189, 0\n\tv_mov_b32 v190, 0\n\tv_mov_b32 v191, 0\n\t"
        ZINIT WL(0) M32(SA0, K1A, "%[q1a]") M8(SA0, K2A, "%[q2a]") "s_nop 15\n\t"
        "LA%=:\n\t"
        CHUNK_HEAD(FCHK)
        PAIR(7*1536, 0, 0, 1, "s_mov_b32 m0, %[mdst]\n\t", DMA(0), VSW,
             "", DMA(1024), DMA(2048), DMA(3072) "s_add_u32 m0, m0, 4096\n\t",
             DMA2(0), "s_mov_b32 exec_hi, 0\n\t" DMA2(1024) "s_mov_b32 exec_hi, -1\n\t")
        "s_cmp_eq_u32 %[ci], 7\n\ts_cselect_b32 %[dlt], %[dwrap], %[dpos]\n\ts_add_u32 %[ci], %[ci], 1\n\ts_and_b32 %[ci], %[ci], 7\n\t"
        "v_add_u32 %[voff], %[dlt], %[voff]\n\tv_add_u32 %[voff2], %[dlt], %[voff2]\n\t"
        "s_add_u32 %[mdst], %[mdst], 22528\n\ts_cmp_eq_u32 %[slot], 1\n\ts_cselect_b32 %[mdst], %[mlo], %[mdst]\n\t"
        "s_sub_u32 %[cnt], %[cnt], 1\n\ts_cmp_lg_u32 %[cnt], 0\n\ts_cbranch_scc1 LA%=\n\t"
        "s_mov_b32 %[cnt], 2\n\t"
        "LB%=:\n\t"
        CHUNK_HEAD("")
        PAIR(7*1536, 0, 0, 1, "", "", VSW, "", "", "", "", "", "")
        "s_sub_u32 %[cnt], %[cnt], 1\n\ts_cmp_lg_u32 %[cnt], 0\n\ts_cbranch_scc1 LB%=\n\t"
        "s_nop 1\n\t" DRAIN "s_waitcnt lgkmcnt(0)\n\ts_nop 15\n\ts_nop 3"
        : [o0] "+v"(o0), [o1] "+v"(o1), [rK] "+v"(rK), [rK2] "+v"(rK2), [rV] "+v"(rV), [voff] "+v"(voff), [voff2] "+v"(voff2), [mdst] "+s"(mdst), [slot] "+s"(slot), [cnt] "+s"(cnt), [dlt] "=&s"(dlt), [ci] "+s"(ci), [f8] "+v"(f8) DOPS
        : [q1a] "v"(q1[0]), [q1b] "v"(q1[1]), [q2a] "v"(q2a), [q2b] "v"(q2b), [idx] "v"(0xDDDDDDDD), [kvp] "s"(kv),
          [dneg] "s"(-2 * KV_CHUNK), [dpos] "s"(KV_CHUNK), [mlo] "s"(mlo), [dwrap] "s"(-7 * KV_CHUNK), [tag] "s"(tag), [fl] "s"(fl), [vF] "v"(FL_CH + (lane & 7) * FS)
        : "memory", "scc", "vcc", KCLOB DCLOB);
    o[0] = o0; o[1] = o1;
}

DI float upper_half(float v) { auto r = __builtin_amdgcn_permlane32_swap(__float_as_uint(v), __float_as_uint(v), false, false); return __uint_as_float(r[1]); }
DI void role_attn(const Params& P, int rb, int cb, unsigned lds0, int w, int lane, Tag& TG SARG) {
    u64 tag, f8;
    __builtin_amdgcn_s_setprio(Q_PRIO);
    const float gt = q_phase(P, rb, cb, lds0, w, lane, TG, tag, f8 SPASS);
    __builtin_amdgcn_s_setprio(A_PRIO);
    const char* kv = P.ws + WS_KV;
    const int n = lane & 31, h = lane >> 5, hp = w >> 1, T = 2 * rb + (w & 1);
    bf16x8 q1[2]; unsigned q2[2];
#pragma unroll
    for (int u = 0; u < 2; ++u) {
        const unsigned a = lds0 + L_Q + (32 * (w & 1) + n) * QROW + 80 * hp + 40 * u;
        q1[u] = cat(*(LAS s16x4*)(a + 16 * h), *(LAS s16x4*)(a + 16 * h + 8)); q2[u] = *(LAS unsigned*)(a + 32 + 4 * h);
    }
    f32x16 o[2];
    STAMP(5);
    attn_fast(kv, P.ws + WS_FL, rb >> 2, tag, f8, lds0, w, lane, q1, q2, o);
    __builtin_amdgcn_s_setprio(3);
    STAMP(6);
    char* O = P.ws + WS_O + (size_t)T * 40960 + (10 * cb + 5 * hp) * 512;
    auto store_o = [&](float l0, float l1) {
        const float i0 = gt * __builtin_amdgcn_rcpf(l0), i1 = gt * __builtin_amdgcn_rcpf(l1);
        unsigned A[3][2], B[3][2];
#pragma unroll
        for (int k = 0; k < 3; ++k) {
            A[k][0] = cvtpk(o[0][4 * k] * i0, o[0][4 * k + 1] * i0); A[k][1] = cvtpk(o[0][4 * k + 2] * i0, o[0][4 * k + 3] * i0);
            B[k][0] = cvtpk(o[1][4 * k] * i1, o[1][4 * k + 1] * i1); B[k][1] = cvtpk(o[1][4 * k + 2] * i1, o[1][4 * k + 3] * i1);
        }
        { auto r0 = __builtin_amdgcn_permlane32_swap(A[0][0], A[1][0], false, false); auto r1 = __builtin_amdgcn_permlane32_swap(A[0][1], A[1][1], false, false);
          st16(O, lane * 16, (u32x4){r0[0], r1[0], r0[1], r1[1]}); }
        if (!h) st16(O, 1024 + n * 16, (u32x4){A[2][0], A[2][1], B[0][0], B[0][1]});
        { auto r0 = __builtin_amdgcn_permlane32_swap(B[0][0], B[2][0], false, false); auto r1 = __builtin_amdgcn_permlane32_swap(B[0][1], B[2][1], false, false);
          st16(O, 1536 + lane * 16, h ? (u32x4){B[1][0], B[1][1], r0[0], r1[0]} : (u32x4){r0[1], r1[1], B[1][0], B[1][1]}); }
    };
    float l0 = upper_half(o[0][8]), l1 = upper_half(o[1][8]);
    float z = 0.f;
#pragma unroll
    for (int r = 0; r < 12; ++r) z = __builtin_fmaf(o[0][r], 0.f, __builtin_fmaf(o[1][r], 0.f, z));
    const bool bad = !(l0 > 0x1p-80f && l0 < 0x1p80f && l1 > 0x1p-80f && l1 < 0x1p80f && z == 0.f);
    if (__builtin_amdgcn_ballot_w64(bad) != 0) {
        attn_safe(kv, lane, q1, q2, o);
        l0 = upper_half(o[0][8]); l1 = upper_half(o[1][8]);
    }
    store_o(l0, l1);
    wait_vm<0>();
    if (lane == 0) __hip_atomic_store((GAS u64*)(P.ws + WS_FL + FL_O + (T * 16 + 2 * cb + hp) * FS), tag, __ATOMIC_RELAXED, __HIP_MEMORY_SCOPE_AGENT);
    STAMP(7);
}

DI void ld8_nowait(u64& v, const char* sbase, unsigned voff) { asm volatile("global_load_dwordx2 %0, %1, %2 sc1" : "=v"(v) : "v"(voff), "s"(sbase) : "memory"); }
DI void ldg3(float (&g)[3], const char* base, int tb, int n) {
#pragma unroll
    for (int t = 0; t < 3; ++t) asm volatile("global_load_dword %0, %1, %2 sc1" : "=v"(g[t]) : "v"((min(3 * tb + t, 63) * 32 + n) * 4), "s"(base) : "memory");
}
DI void pin1(u64& f) { asm volatile("" : "+v"(f)); }
DI void pin3(float (&g)[3]) { asm volatile("" : "+v"(g[0]), "+v"(g[1]), "+v"(g[2])); }
struct OutCtx { unsigned voffW[4], ldsw; int rot; const char *wd, *wo; };
DI void out_stageW(const OutCtx& C, int st, int w) {
    const int v = st + C.rot;
    const char* b = C.wd + (unsigned)(v >= 12 ? v - 12 : v) * 40960u;
    set_m0(C.ldsw + (st % 5) * 4096); dma<0>(b, C.voffW[0]); dma<1024>(b, C.voffW[1]); dma<2048>(b, C.voffW[2]); dma<3072>(b, C.voffW[3]);
}
DI void out_init(OutCtx& C, const Params& P, int tb, int cj, unsigned lds0, int w, int lane) {
#pragma unroll
    for (int j = 0; j < 4; ++j) C.voffW[j] = (8 * j + (lane >> 3)) * 1280 + (((lane & 7) ^ (((lane >> 4) & 1) << 2)) * 16) + 3072 - 1024 * j;
    C.ldsw = lds0 + w * 20480; C.rot = (cj + 4 * (tb % 3)) % 12;
    C.wd = (const char*)P.wd + (size_t)(12 * w) * 40960 + cj * 128 - 3072; C.wo = (const char*)P.wo + (size_t)(5 * w) * 40960 + cj * 128 - 3072;
}
DI unsigned gelu_mul2(f32x2 a, f32x2 u) {
    const f32x2 z = a * __builtin_elementwise_fma(a * a, (f32x2){-0.1029432397f, -0.1029432397f}, (f32x2){-2.302208198f, -2.302208198f});
    const f32x2 d = (f32x2){__builtin_amdgcn_exp2f(z[0]), __builtin_amdgcn_exp2f(z[1])} + (f32x2){1.f, 1.f};
    const f32x2 r = (a * u) * (f32x2){__builtin_amdgcn_rcpf(d[0]), __builtin_amdgcn_rcpf(d[1])};
    return cvtpk(r[0], r[1]);
}
template <class F> DI void ffnup_phase(const Params& P, int RB, int CB, int rot, unsigned lds0, int w, int lane, u64 tag, bool more, F&& mid SARG) {
    constexpr int L_W = 32768, WSL = 12288;
    const int wn = w & 1, wf = w >> 1;
    unsigned voffW[3], voffX[4];
#pragma unroll
    for (int j = 0; j < 3; ++j) { const int b = ((w & 1) * 3 + j) * 1024 + 16 * lane, r = b / 192, ch = ((b % 192) >> 4) ^ ((w >> 1) ? 2 : 0); voffW[j] = r * 3072 + ch * 16 + 2048 - 1024 * j; }
#pragma unroll
    for (int j = 0; j < 4; ++j) voffX[j] = (8 * j + (lane >> 3)) * 1280 + (((lane & 7) ^ ((lane >> 4) & 3) ^ ((j & 1) * 4)) * 16) + 3072 - 1024 * j;
    const char* baseW = (const char*)((w >> 1) ? P.wu : P.wg) + CB * 192 - 2048;
    const char* baseX = (const char*)P.x + (size_t)(128 * RB + 32 * w) * 1280 - 3072;
    const unsigned mW = __builtin_amdgcn_readfirstlane(lds0 + L_W + w * 3072), mX = __builtin_amdgcn_readfirstlane(lds0 + w * 4096);
    int kW = rot * 2, kX = rot;
    auto nextW = [&]() { const char* b = baseW + (unsigned)kW * (32u * 3072u); kW = kW == 19 ? 0 : kW + 1; return b; };
    auto nextX = [&]() { const char* b = baseX + (unsigned)kX * 128u; kX = kX == 9 ? 0 : kX + 1; return b; };
    { const char* b = nextX(); set_m0(mX); SBAR(); dma<0>(b, voffX[0]); dma<1024>(b, voffX[1]); dma<2048>(b, voffX[2]); dma<3072>(b, voffX[3]); }
#pragma unroll
    for (int i = 0; i < 3; ++i) { const char* b = nextW(); set_m0(mW + i * WSL); SBAR(); dma<0>(b, voffW[0]); dma<1024>(b, voffW[1]); dma<2048>(b, voffW[2]); }
    const char* bX = nextX();
    set_m0(mX + 16384); SBAR(); dma<0>(bX, voffX[0]); dma<1024>(bX, voffX[1]);
    const int h = lane >> 5, g = (lane >> 4) & 1, q = (lane >> 2) & 3, p = lane & 3, n = lane & 31;
    unsigned rB[4], rAw[3];
#pragma unroll
    for (int s = 0; s < 4; ++s) rB[s] = lds0 + (64 * wn + n) * 128 + (((2 * s + h) ^ ((n >> 1) & 7)) * 16);
#pragma unroll
    for (int a = 0; a < 3; ++a) { const int T = 3 * wf + a; rAw[a] = lds0 + L_W + g * 6144 + (8 * h + q) * 192 + (((T & 1) ^ g) * 32) + 8 * p + (T >> 1) * 64; }
    f32x16 acc[3][2];
#pragma unroll
    for (int a = 0; a < 3; ++a)
#pragma unroll
        for (int b = 0; b < 2; ++b)
#pragma unroll
            for (int r = 0; r < 16; ++r) acc[a][b][r] = 0.f;
    bf16x8 A0[3], B0[2], A1[3], B1[2];
    auto rdB = [&](bf16x8 (&B)[2], int i, int s) {
#pragma unroll
        for (int b = 0; b < 2; ++b) B[b] = lds128(rB[2 * (i & 1) + s] + ((i >> 1) & 1) * 16384 + b * 4096); };
    auto rdA = [&](bf16x8 (&A)[3], int a, int i, int s) { const unsigned ad = rAw[a] + (i & 3) * WSL + 16 * s * 192; A[a] = cat(trd(ad), trd(ad + 768)); };
    wait_bar<8>();
    rdB(B0, 0, 0); rdA(A0, 0, 0, 0); rdA(A0, 1, 0, 0); rdA(A0, 2, 0, 0);
    SBAR();
#define MM(A, B, a, b) acc[a][b] = mfma32(A[a], B[b], acc[a][b])
    auto stagef = [&](auto I, auto LW, auto LX, auto NV, auto NXT) {
        constexpr int i = I; constexpr bool lw = LW, lx = LX, nx = NXT; constexpr int nv = NV;
        const char* bW = nullptr;
        MM(A0, B0, 0, 0); rdB(B1, i, 1); SBAR();
        MM(A0, B0, 0, 1); rdA(A1, 0, i, 1); SBAR();
        MM(A0, B0, 1, 0); rdA(A1, 1, i, 1); if constexpr (lx && !(i & 1)) { set_m0(mX + (((i >> 1) + 1) & 1) * 16384); } SBAR();
        MM(A0, B0, 1, 1); rdA(A1, 2, i, 1); if constexpr (lx && !(i & 1)) { dmap<2048, UP_PRIO, UP_BOOST>(bX, voffX[2]); dmap<3072, UP_PRIO, UP_BOOST>(bX, voffX[3]); } if constexpr (lw) { bW = nextW(); set_m0(mW + ((i + 3) & 3) * WSL); } SBAR();
        MM(A0, B0, 2, 0); if constexpr (lw) dmap<0, UP_PRIO, UP_BOOST>(bW, voffW[0]); SBAR();
        MM(A0, B0, 2, 1); if constexpr (lw) dmap<1024, UP_PRIO, UP_BOOST>(bW, voffW[1]); SBAR();
        if constexpr (nx) wait_bar<nv>(); else wait_lgkm0();
        SBAR();
        MM(A1, B1, 0, 0); if constexpr (nx) rdB(B0, i + 1, 0); SBAR();
        MM(A1, B1, 0, 1); if constexpr (nx) rdA(A0, 0, i + 1, 0); SBAR();
        MM(A1, B1, 1, 0); if constexpr (nx) rdA(A0, 1, i + 1, 0); if constexpr (lw) dmap<2048, UP_PRIO, UP_BOOST>(bW, voffW[2]); SBAR();
        MM(A1, B1, 1, 1); if constexpr (nx) rdA(A0, 2, i + 1, 0); if constexpr (lx && (i & 1)) { bX = nextX(); set_m0(mX + (((i >> 1) + 2) & 1) * 16384); } SBAR();
        MM(A1, B1, 2, 0); if constexpr (lx && (i & 1)) dmap<0, UP_PRIO, UP_BOOST>(bX, voffX[0]); SBAR();
        MM(A1, B1, 2, 1); if constexpr (lx && (i & 1)) dmap<1024, UP_PRIO, UP_BOOST>(bX, voffX[1]); SBAR();
    };
    using T_ = std::true_type; using F_ = std::false_type;
#pragma nounroll
    for (int it = 0; it < 4; ++it) {
        stagef(IC(0), T_{}, T_{}, IC(9), T_{}); stagef(IC(1), T_{}, T_{}, IC(5), T_{}); stagef(IC(2), T_{}, T_{}, IC(9), T_{}); stagef(IC(3), T_{}, T_{}, IC(5), T_{});
    }
    stagef(IC(0), T_{}, T_{}, IC(9), T_{}); stagef(IC(1), F_{}, F_{}, IC(3), T_{}); stagef(IC(2), F_{}, F_{}, IC(0), T_{}); stagef(IC(3), F_{}, F_{}, IC(0), F_{});
#undef MM
    STAMP(2);
    HI();
    u32x4 hg[3][2];
#pragma unroll
    for (int a = 0; a < 3; ++a)
#pragma unroll
        for (int b = 0; b < 2; ++b) {
            const f32x16& c = acc[a][b];
            const unsigned a0 = gelu_mul2((f32x2){c[0], c[1]}, (f32x2){c[8], c[9]}), a1 = gelu_mul2((f32x2){c[2], c[3]}, (f32x2){c[10], c[11]});
            const unsigned b0 = gelu_mul2((f32x2){c[4], c[5]}, (f32x2){c[12], c[13]}), b1 = gelu_mul2((f32x2){c[6], c[7]}, (f32x2){c[14], c[15]});
            auto r0 = __builtin_amdgcn_permlane32_swap(a0, b0, false, false);
            auto r1 = __builtin_amdgcn_permlane32_swap(a1, b1, false, false);
            hg[a][b] = (u32x4){r0[0], r1[0], r0[1], r1[1]}; asm volatile("" : "+v"(hg[a][b]));
        }
    __builtin_amdgcn_s_setprio(ST_PRIO);
#pragma unroll
    for (int a = 0; a < 3; ++a)
#pragma unroll
        for (int b = 0; b < 2; ++b)
            st16(P.ws + WS_H + (size_t)(4 * RB + 2 * wn + b) * 98304 + (6 * CB + 3 * wf + a) * 1024, lane * 16, hg[a][b]);
    bar(); mid();
    if (more) wait_vm<20>(); else wait_vm<0>();
    bar();
    if (threadIdx.x == 0) __hip_atomic_store((GAS u64*)(P.ws + WS_FL + FL_H + (RB * 16 + CB) * FS), tag, __ATOMIC_RELAXED, __HIP_MEMORY_SCOPE_AGENT);
}

DI void out_phase(const Params& P, const OutCtx& C, int tb, int cj, unsigned lds0, int w, int lane, u64 tag SARG) {
    const int n = lane & 31, h = lane >> 5, mh = (lane >> 4) & 1, q = (lane >> 2) & 3, p = lane & 3;
    const unsigned ldsw = C.ldsw;
    const char* fl = P.ws + WS_FL;
    unsigned vH[3], vO[3];
#pragma unroll
    for (int t = 0; t < 3; ++t) { const int T = min(3 * tb + t, 63); vH[t] = T * 98304 + lane * 16; vO[t] = T * 40960 + lane * 16; }
#define LDB(dst, base, vo, off) asm volatile("global_load_dwordx4 %0, %1, %2 offset:" #off " sc1" : "=v"(dst) : "v"(vo), "s"(base) : "memory")
#define LDBP(dst, base, vo, off) asm volatile("s_setprio " STR(D_BOOST) "\n\tglobal_load_dwordx4 %0, %1, %2 offset:" #off " sc1\n\ts_setprio " STR(G_PRIO) : "=v"(dst) : "v"(vo), "s"(base) : "memory")
#define PIN6(b) asm volatile("" : "+v"(b[0]), "+v"(b[1]), "+v"(b[2]), "+v"(b[3]), "+v"(b[4]), "+v"(b[5]))
    unsigned rA[2];
#pragma unroll
    for (int t = 0; t < 2; ++t) rA[t] = ldsw + (8 * h + q) * 128 + ((t ^ (q >> 1)) * 64) + mh * 32 + 8 * p;
    f32x16 acc[2][3];
#pragma unroll
    for (int a = 0; a < 2; ++a)
#pragma unroll
        for (int b = 0; b < 3; ++b)
#pragma unroll
            for (int r = 0; r < 16; ++r) acc[a][b][r] = 0.f;
    bf16x8 bf[3][6], af[2][4];
    auto rdA = [&](bf16x8 (&A)[4], int f, int slot) { const unsigned a = rA[f & 1] + slot * 4096 + (f >> 1) * 2048; A[f] = cat(trd(a), trd(a + 512)); };
    const char* Hb = P.ws + WS_H + 12 * w * 2048;
    auto hbase = [&](int st) { const int v = st + C.rot; return Hb + (unsigned)(v >= 12 ? v - 12 : v) * 2048u; };
    auto loadB = [&](const char* base, const unsigned (&vo)[3], bf16x8 (&b)[6]) { LDB(b[0], base, vo[0], 0); LDB(b[1], base, vo[1], 0); LDB(b[2], base, vo[2], 0); LDB(b[3], base, vo[0], 1024); LDB(b[4], base, vo[1], 1024); LDB(b[5], base, vo[2], 1024); };
    __builtin_amdgcn_s_setprio(0);
    { const int r0 = (96 * tb) >> 7, r1 = min((96 * tb + 95) >> 7, 15); wait_flags(fl, FL_H + (((lane & 4) ? r1 : r0) * 16 + 4 * w + (lane & 3)) * FS, tag); }
    __builtin_amdgcn_s_setprio(D_BOOST);
    STAMP(4);
    loadB(hbase(0), vH, bf[0]); loadB(hbase(1), vH, bf[1]);
    __builtin_amdgcn_s_setprio(G_PRIO);
    rdA(af[0], 0, 0); rdA(af[0], 1, 0); rdA(af[0], 2, 0); rdA(af[0], 3, 0);
    wait_vm<6>();
#define MM(f, tt) acc[(f) & 1][tt] = mfma32(A[f], b[3 * ((f) >> 1) + tt], acc[(f) & 1][tt])
    auto step = [&](auto ST, auto LB, auto LW, auto RN, const char* nb, const unsigned (&vo)[3], auto BI) {
        constexpr int st = ST, bi = BI; constexpr bool lb = LB, lw = LW, rn = RN;
        bf16x8(&b)[6] = bf[st % 3]; bf16x8(&A)[4] = af[st & 1]; bf16x8(&An)[4] = af[(st + 1) & 1]; bf16x8(&bn)[6] = bf[bi];
        PIN6(b); SBAR();
        MM(0, 0); if constexpr (lb) LDBP(bn[0], nb, vo[0], 0); SBAR();
        MM(0, 1); if constexpr (rn) rdA(An, 0, (st + 1) % 5); SBAR();
        MM(0, 2); if constexpr (lb) LDBP(bn[1], nb, vo[1], 0); SBAR();
        MM(1, 0); if constexpr (rn) rdA(An, 1, (st + 1) % 5); SBAR();
        MM(1, 1); if constexpr (lb) LDBP(bn[2], nb, vo[2], 0); SBAR();
        MM(1, 2); if constexpr (rn) rdA(An, 2, (st + 1) % 5); SBAR();
        MM(2, 0); if constexpr (lb) LDBP(bn[3], nb, vo[0], 1024); SBAR();
        MM(2, 1); if constexpr (rn) rdA(An, 3, (st + 1) % 5); if constexpr (lb) LDBP(bn[4], nb, vo[1], 1024); SBAR();
        const char* wb = nullptr;
        MM(2, 2); if constexpr (lb) LDBP(bn[5], nb, vo[2], 1024);
        if constexpr (lw) { const int v = st + 5 + C.rot; wb = st + 5 < 12 ? C.wd + (unsigned)(v >= 12 ? v - 12 : v) * 40960u : C.wo + (st + 5 - 12) * 40960; set_m0(ldsw + (st % 5) * 4096); dmap<0, G_PRIO, D_BOOST>(wb, C.voffW[0]); } SBAR();
        MM(3, 0); if constexpr (lw) dmap<1024, G_PRIO, D_BOOST>(wb, C.voffW[1]); SBAR();
        MM(3, 1); if constexpr (lw) dmap<2048, G_PRIO, D_BOOST>(wb, C.voffW[2]); SBAR();
        MM(3, 2); if constexpr (lw) dmap<3072, G_PRIO, D_BOOST>(wb, C.voffW[3]); SBAR();
    };
    const char* Ob = P.ws + WS_O + 5 * w * 2048;
    const unsigned vFo = FL_O + (min(3 * tb + ((lane >> 2) & 3) % 3, 63) * 16 + 4 * w + (lane & 3)) * FS;
    u64 fo; float gs[3];
    sfor<0, 17>([&](auto SI) {
        constexpr int st = SI;
        if constexpr (st == 12) {
            STAMP(5);
            pin3(gs);
#pragma unroll
            for (int a = 0; a < 2; ++a)
#pragma unroll
                for (int b = 0; b < 3; ++b) acc[a][b] *= 1.f - gs[b];
            HI();
        }
        if constexpr (st + 2 < 12) step(SI, std::true_type{}, std::true_type{}, std::true_type{}, hbase(st + 2), vH, IC((st + 2) % 3));
        else step(SI, std::integral_constant<bool, (st + 2 < 17)>{}, std::integral_constant<bool, (st + 5 < 17)>{}, std::integral_constant<bool, (st + 1 < 17)>{}, Ob + (st + 2 - 12) * 2048, vO, IC((st + 2) % 3));
        if constexpr (st == 8) ld8_nowait(fo, fl, vFo);
        constexpr int later = (st >= 1 && st + 4 < 17 ? 4 : 0) + (st + 2 < 17 ? 6 : 0) + (st + 5 < 17 ? 4 : 0);
        if constexpr (st == 8) wait_vm<later + 1>();
        else if constexpr (st == 9) {
            wait_vm<10>(); pin1(fo);
            if (__builtin_amdgcn_ballot_w64(fo != tag) != 0) { __builtin_amdgcn_s_setprio(0); wait_flags<2>(fl, vFo, tag); __builtin_amdgcn_s_setprio(G_PRIO); }
            ldg3(gs, P.ws + WS_G, tb, n);
            STAMP(6);
        } else if constexpr (st == 10) wait_vm<later + 3>();
        else if constexpr (st + 1 < 17) wait_vm<later>();
    });
#undef MM
    STAMP(7);
    bar();
    sfor<0, 4>([&](auto V) {
        constexpr int v = V;
        if (w == v) {
#pragma unroll
            for (int d = 0; d < 4; ++d) if (d != v) {
                const unsigned dst = lds0 + d * 20480 + (v > d ? v - 1 : v) * 6144 + lane * 16;
#pragma unroll
                for (int a = 0; a < 2; ++a)
#pragma unroll
                    for (int b = 0; b < 3; ++b) *(LAS f32x4*)(dst + (a * 3 + b) * 1024) = (f32x4){acc[a][b][4 * d], acc[a][b][4 * d + 1], acc[a][b][4 * d + 2], acc[a][b][4 * d + 3]};
            }
            bar();
#pragma unroll
            for (int b = 0; b < 3; ++b) {
                f32x4 res[2];
#pragma unroll
                for (int a = 0; a < 2; ++a) { res[a] = (f32x4){acc[a][b][4 * v], acc[a][b][4 * v + 1], acc[a][b][4 * v + 2], acc[a][b][4 * v + 3]};
#pragma unroll
                    for (int k = 0; k < 3; ++k) res[a] += *(LAS f32x4*)(ldsw + k * 6144 + (a * 3 + b) * 1024 + lane * 16); }
                auto r0 = __builtin_amdgcn_permlane32_swap(cvtpk(res[0][0], res[0][1]), cvtpk(res[1][0], res[1][1]), false, false);
                auto r1 = __builtin_amdgcn_permlane32_swap(cvtpk(res[0][2], res[0][3]), cvtpk(res[1][2], res[1][3]), false, false);
                if (3 * tb + b < 64) *(GAS u32x4*)(P.out + (size_t)(32 * (3 * tb + b) + n) * D_ + 64 * cj + 32 * h + 8 * v) = (u32x4){r0[0], r1[0], r0[1], r1[1]};
            }
        }
    });
}

__global__ __launch_bounds__(256, 2) void fused_kernel(Params P) {
    extern __shared__ __attribute__((aligned(16))) char lds[];
    const unsigned lds0 = (unsigned)(uintptr_t)(LAS char*)lds;
    const int lane = threadIdx.x & 63, w = __builtin_amdgcn_readfirstlane(threadIdx.x >> 6);
    const int b = blockIdx.x & 255, xc = b & 7, idx = b >> 3;
    STAMP(0);
    asm volatile("" ::"s"(P.x), "s"(P.wq), "s"(P.wk), "s"(P.wv), "s"(P.wo), "s"(P.wg), "s"(P.wu), "s"(P.wd), "s"(P.gw), "s"(P.out), "s"(P.ws));
    Tag TG; TG.issue(P.ws + WS_FL);
    if (blockIdx.x < 256) role_attn(P, 4 * xc + (idx >> 3), idx & 7, lds0, w, lane, TG SPASS);
    else {
        __builtin_amdgcn_s_setprio(3);
        kv_job(P, 16 * xc + (idx >> 1), idx & 1, lds0, w, lane SPASS);
        const u64 tag = TG.get();
        STAMP(8);
        publish(P.ws + WS_FL, FL_KV + (32 * xc + idx) * FS, tag);
        if (idx == 0 && w == 0) {
            wait_flags<1>(P.ws + WS_FL, FL_KV + (32 * xc + (lane & 31)) * FS, tag);
            if (lane == 0) __hip_atomic_store((GAS u64*)(P.ws + WS_FL + FL_CH + xc * FS), tag, __ATOMIC_RELAXED, __HIP_MEMORY_SCOPE_AGENT);
        }
        __builtin_amdgcn_s_setprio(UP_PRIO);
        STAMP(9);
        if (G_DELAY) __builtin_amdgcn_s_sleep(G_DELAY);
        STAMP(1);
        const int l0 = (220 * xc) >> 3, l1 = (220 * (xc + 1)) >> 3, L = l0 + idx;
        OutCtx C;
        ffnup_phase(P, 8 * (xc >> 2) + (idx >> 2), 4 * (xc & 3) + (idx & 3), ((idx >> 2) + 3 * (idx & 3)) % 10, lds0, w, lane, tag, L < l1,
                    [&]() { out_init(C, P, L / 10, L % 10, lds0, w, lane); if (L < l1) { out_stageW(C, 0, w); out_stageW(C, 1, w); out_stageW(C, 2, w); out_stageW(C, 3, w); out_stageW(C, 4, w); } } SPASS);
        STAMP(3);
        if (L < l1) out_phase(P, C, L / 10, L % 10, lds0, w, lane, tag SPASS);
        else if (b == 255 && w == 0) {
            __builtin_amdgcn_s_setprio(0);
#pragma nounroll
            for (int i = 0; i < 4; ++i) wait_flags<32>(P.ws + WS_FL, FL_H + (i * 64 + lane) * FS, tag);
#pragma nounroll
            for (int i = 0; i < 4; ++i) wait_flags<32>(P.ws + WS_FL, FL_O + ((i * 64 + lane) >> 3) * 32 * FS + ((i * 64 + lane) & 7) * 2 * FS, tag);
            if (lane == 0) __hip_atomic_store((GAS u64*)(P.ws + WS_FL), tag, __ATOMIC_RELAXED, __HIP_MEMORY_SCOPE_AGENT);
        }
    }
}

extern "C" void kernel_launch(void* const* d_in, const int* in_sizes, int n_in, void* d_out, int out_size, void* d_ws, size_t ws_size, hipStream_t stream) {
    Params P;
    P.x = (const u16*)d_in[0]; P.wq = (const u16*)d_in[1]; P.wk = (const u16*)d_in[2]; P.wv = (const u16*)d_in[3]; P.wo = (const u16*)d_in[4];
    P.wg = (const u16*)d_in[6]; P.wu = (const u16*)d_in[7]; P.wd = (const u16*)d_in[8]; P.gw = (const u16*)d_in[9];
    P.out = (u16*)d_out; P.ws = (char*)d_ws;
    (void)hipFuncSetAttribute((const void*)fused_kernel, hipFuncAttributeMaxDynamicSharedMemorySize, LDS_BYTES);
    fused_kernel<<<512, 256, LDS_BYTES, stream>>>(P);
}
```
